```python
import torch

def _softmax(x, axis=-1):
    return torch.softmax(x, dim=axis)
def _gelu(x):
    return torch.nn.functional.gelu(x, approximate="tanh")
def _silu(x):
    return torch.nn.functional.silu(x)
def _sigmoid(x):
    return torch.sigmoid(x)
def _layernorm(x, axis=-1, eps=1e-5):
    m = x.mean(dim=axis, keepdim=True)
    v = x.var(dim=axis, keepdim=True, unbiased=False)
    return (x - m) / torch.sqrt(v + eps)
def _scatter_add(tgt, idx, src, axis=0):
    return tgt.clone().index_add_(axis, idx, src)
def _segment_sum(src, seg, n):
    out = torch.zeros((n,) + src.shape[1:], dtype=src.dtype)
    return out.index_add_(0, seg, src)
def _pad(x, pw):
    flat = tuple(v for pair in reversed(pw) for v in pair)
    return torch.nn.functional.pad(x, flat)

kernel_name = 'concat_heads__mha_block_cached__gqa_block_cached__near_ridge'

def setup_inputs():

    _w = (lambda a: (lambda t: t.to(torch.bfloat16) if t.is_floating_point() else t)(torch.as_tensor(a)))
    def _A_setup():
        B, Sq, D = 8, 144, 512
        H, Sk, Dh = 32, 512, 128
        return {
            "x": _w((lambda *s: torch.randn(*s, dtype=torch.bfloat16) * 0.8)(B, Sq, D)),
            "k_cache": _w((lambda *s: torch.randn(*s, dtype=torch.bfloat16) * 0.8)(B, H, Sk, Dh)),
            "v_cache": _w((lambda *s: torch.randn(*s, dtype=torch.bfloat16) * 0.05)(B, H, Sk, Dh)),
            "wq": _w((lambda *s: torch.randn(*s, dtype=torch.bfloat16) * 0.8)(D, H * Dh) * (D ** -0.5)),
            "wo": _w((lambda *s: torch.randn(*s, dtype=torch.bfloat16) * 0.05)(H * Dh, D) * ((H * Dh) ** -0.5)),
        }
    _A_in = _A_setup()

    def _B_setup():
        B, Sq, D = 8, 144, 512
        Hq, Hkv, Sk, Dh = 32, 32, 512, 128
        assert Hq % Hkv == 0, (Hq, Hkv)
        return {
            "x": _w((lambda *s: torch.randn(*s, dtype=torch.bfloat16) * 0.8)(B, Sq, D)),
            "k_cache": _w((lambda *s: torch.randn(*s, dtype=torch.bfloat16) * 0.8)(B, Hkv, Sk, Dh)),
            "v_cache": _w((lambda *s: torch.randn(*s, dtype=torch.bfloat16) * 0.05)(B, Hkv, Sk, Dh)),
            "wq": _w((lambda *s: torch.randn(*s, dtype=torch.bfloat16) * 0.8)(D, Hq * Dh) * (D ** -0.5)),
            "wo": _w((lambda *s: torch.randn(*s, dtype=torch.bfloat16) * 0.05)(Hq * Dh, D) * ((Hq * Dh) ** -0.5)),
        }
    _B_in = _B_setup()

    return {
        "A_x": _A_in["x"],
        "A_k_cache": _A_in["k_cache"],
        "A_v_cache": _A_in["v_cache"],
        "A_wq": _A_in["wq"],
        "A_wo": _A_in["wo"],
        "B_x": _B_in["x"],
        "B_k_cache": _B_in["k_cache"],
        "B_v_cache": _B_in["v_cache"],
        "B_wq": _B_in["wq"],
        "B_wo": _B_in["wo"],
    }

def reference(A_x, A_k_cache, A_v_cache, A_wq, A_wo, B_x, B_k_cache, B_v_cache, B_wq, B_wo):
    def _A_ref(x, k_cache, v_cache, wq, wo):
        B, Sq, D = x.shape
        H, Dh = 32, 128
        q = (lambda x, *d: x.permute(*d))((x @ wq).reshape(B, Sq, H, Dh), 0, 2, 1, 3)
        scores = torch.einsum("bhqd,bhkd->bhqk", q, k_cache) * (Dh ** -0.5)
        p = _softmax(scores, axis=-1)
        o = torch.einsum("bhqk,bhkd->bhqd", p, v_cache)
        return (lambda x, *d: x.permute(*d))(o, 0, 2, 1, 3).reshape(B, Sq, H * Dh) @ wo

    def _B_ref(x, k_cache, v_cache, wq, wo):
        B, Sq, D = x.shape
        Hq, Dh = 32, 128
        Hkv = k_cache.shape[1]
        rep = Hq // Hkv
        q = (lambda x, *d: x.permute(*d))((x @ wq).reshape(B, Sq, Hq, Dh), 0, 2, 1, 3)
        k = (lambda x, n, axis: torch.repeat_interleave(x, n, dim=axis))(k_cache, rep, axis=1)
        v = (lambda x, n, axis: torch.repeat_interleave(x, n, dim=axis))(v_cache, rep, axis=1)
        scores = torch.einsum("bhqd,bhkd->bhqk", q, k) * (Dh ** -0.5)
        p = _softmax(scores, axis=-1)
        o = torch.einsum("bhqk,bhkd->bhqd", p, v)
        return (lambda x, *d: x.permute(*d))(o, 0, 2, 1, 3).reshape(B, Sq, Hq * Dh) @ wo

    a = _A_ref(A_x, A_k_cache, A_v_cache, A_wq, A_wo)
    b = _B_ref(B_x, B_k_cache, B_v_cache, B_wq, B_wo)
    return torch.concatenate([a, b], axis=-1)
```

```cpp
#include <hip/hip_runtime.h>
#include <hip/hip_bf16.h>
#include <stdint.h>

typedef short    s16x4  __attribute__((ext_vector_type(4)));
typedef short    s16x8  __attribute__((ext_vector_type(8)));
typedef __bf16   bf16x2 __attribute__((ext_vector_type(2)));
typedef __bf16   bf16x8 __attribute__((ext_vector_type(8)));
typedef float    f32x2  __attribute__((ext_vector_type(2)));
typedef float    f32x4  __attribute__((ext_vector_type(4)));
typedef unsigned u32x2  __attribute__((ext_vector_type(2)));
typedef unsigned u32x4  __attribute__((ext_vector_type(4)));
typedef __attribute__((address_space(3))) s16x4* lds_s16x4_p;
typedef __attribute__((address_space(3))) char* lds_char_p;

#define DI __device__ __forceinline__

constexpr int NB = 8, SQ = 144, DM = 512, NH = 32, SK = 512, DH = 128, HD = NH * DH;
constexpr int NQT = SQ / 16;
constexpr int TILE_B = 64 * 256;
constexpr int LDS1 = 5 * TILE_B;
constexpr int LDS2 = 8 * 20480;
constexpr float QSCALE = 0.08838834764831845f * 1.4426950408889634f;
constexpr float RESCALE_THR = 8.0f;

DI f32x4 mfma32(bf16x8 a, bf16x8 b, f32x4 c) { return __builtin_amdgcn_mfma_f32_16x16x32_bf16(a, b, c, 0, 0, 0); }
DI f32x4 mfma16(s16x4 a, s16x4 b, f32x4 c) { return __builtin_amdgcn_mfma_f32_16x16x16bf16_1k(a, b, c, 0, 0, 0); }
DI unsigned pk2(float lo, float hi) { f32x2 v = {lo, hi}; return __builtin_bit_cast(unsigned, __builtin_convertvector(v, bf16x2)); }
DI s16x4 tr_read(lds_char_p p) { return __builtin_amdgcn_ds_read_tr16_b64_v4i16((lds_s16x4_p)p); }
DI bf16x8 tr_frag(lds_char_p p, int second) {
  s16x4 lo = tr_read(p), hi = tr_read(p + second);
  return __builtin_bit_cast(bf16x8, __builtin_shufflevector(lo, hi, 0, 1, 2, 3, 4, 5, 6, 7));
}
DI bf16x8 pack8(f32x4 a, f32x4 b) {
  u32x4 w = {pk2(a[0], a[1]), pk2(a[2], a[3]), pk2(b[0], b[1]), pk2(b[2], b[3])};
  return __builtin_bit_cast(bf16x8, w);
}
DI float max3(float a, float b, float c) { float r; asm("v_max3_f32 %0, %1, %2, %3" : "=v"(r) : "v"(a), "v"(b), "v"(c)); return r; }
DI float xg_max(float x) {
  auto a = __builtin_amdgcn_permlane32_swap(__float_as_uint(x), __float_as_uint(x), false, false);
  x = fmaxf(__uint_as_float(a[0]), __uint_as_float(a[1]));
  auto b = __builtin_amdgcn_permlane16_swap(__float_as_uint(x), __float_as_uint(x), false, false);
  return fmaxf(__uint_as_float(b[0]), __uint_as_float(b[1]));
}
DI float xg_sum(float x) {
  auto a = __builtin_amdgcn_permlane32_swap(__float_as_uint(x), __float_as_uint(x), false, false);
  x = __uint_as_float(a[0]) + __uint_as_float(a[1]);
  auto b = __builtin_amdgcn_permlane16_swap(__float_as_uint(x), __float_as_uint(x), false, false);
  return __uint_as_float(b[0]) + __uint_as_float(b[1]);
}

DI void dma4(unsigned lds, const char* base, unsigned v0, unsigned v1, unsigned v2, unsigned v3) {
  unsigned keep;
  asm volatile(
      "s_mov_b32 %0, m0\n\t"
      "s_mov_b32 m0, %1\n\t"
      "s_nop 0\n\t"
      "global_load_lds_dwordx4 %3, %2\n\t"
      "global_load_lds_dwordx4 %4, %2 offset:1024\n\t"
      "global_load_lds_dwordx4 %5, %2 offset:2048\n\t"
      "global_load_lds_dwordx4 %6, %2 offset:3072\n\t"
      "s_mov_b32 m0, %0"
      : "=&s"(keep) : "s"(lds), "s"(base), "v"(v0), "v"(v1), "v"(v2), "v"(v3) : "memory");
}
template <int OFF> DI void gload16(bf16x8& dst, const char* base, unsigned voff) {
  asm volatile("global_load_dwordx4 %0, %1, %2 offset:%3" : "=v"(dst) : "v"(voff), "s"(base), "i"(OFF) : "memory");
}
#define WAIT_VM(n) asm volatile("s_waitcnt vmcnt(" #n ")" ::: "memory")
#define BARRIER() asm volatile("s_waitcnt lgkmcnt(0)\n\ts_barrier" ::: "memory")
#define SCHED_FENCE() __builtin_amdgcn_sched_barrier(0)

struct XSet { bf16x8 f[3][2]; };

DI void x_issue(XSet& s, const char* xb, unsigned vo, unsigned v9) {
  gload16<0>(s.f[0][0], xb, vo);
  gload16<64>(s.f[0][1], xb, vo);
  gload16<0>(s.f[1][0], xb + 16 * DM * 2, vo);
  gload16<64>(s.f[1][1], xb + 16 * DM * 2, vo);
  gload16<0>(s.f[2][0], xb, v9);
  gload16<64>(s.f[2][1], xb, v9);
}
DI void x_pin(XSet& s) {
  asm volatile("" : "+v"(s.f[0][0]), "+v"(s.f[0][1]), "+v"(s.f[1][0]), "+v"(s.f[1][1]), "+v"(s.f[2][0]), "+v"(s.f[2][1]));
}
DI bf16x8 q_frag(f32x4 t0, f32x4 t1) {
  unsigned a0 = pk2(t0[0] * QSCALE, t0[1] * QSCALE), a1 = pk2(t0[2] * QSCALE, t0[3] * QSCALE);
  unsigned b0 = pk2(t1[0] * QSCALE, t1[1] * QSCALE), b1 = pk2(t1[2] * QSCALE, t1[3] * QSCALE);
  auto p0 = __builtin_amdgcn_permlane32_swap(a0, b0, false, false);
  auto p1 = __builtin_amdgcn_permlane32_swap(a1, b1, false, false);
  auto r0 = __builtin_amdgcn_permlane16_swap(p0[0], p0[1], false, false);
  auto r1 = __builtin_amdgcn_permlane16_swap(p1[0], p1[1], false, false);
  u32x4 w = {r0[0], r1[0], r0[1], r1[1]};
  return __builtin_bit_cast(bf16x8, w);
}

__global__ __launch_bounds__(256, 2)
void qattn_kernel(const char* __restrict__ xA, const char* __restrict__ kA, const char* __restrict__ vA, const char* __restrict__ wqA,
                  const char* __restrict__ xB, const char* __restrict__ kB, const char* __restrict__ vB, const char* __restrict__ wqB,
                  char* __restrict__ ows) {
  extern __shared__ __attribute__((aligned(16))) char smem_raw[];
  lds_char_p smem = (lds_char_p)smem_raw;
  const unsigned lds0 = (unsigned)(uintptr_t)smem;
  const int tid = threadIdx.x, lane = tid & 63, r = lane & 15, g = lane >> 4;
  const int w = __builtin_amdgcn_readfirstlane(tid >> 6);
  const int bid = blockIdx.x, xcd = bid & 7, idx = bid >> 3;
  const int br = xcd >> 2, b = 2 * (xcd & 3) + (idx & 1), h = idx >> 1;
  const char* xg = (br ? xB : xA) + (size_t)b * SQ * DM * 2;
  const char* kg = (br ? kB : kA) + (size_t)(b * NH + h) * SK * DH * 2;
  const char* vg = (br ? vB : vA) + (size_t)(b * NH + h) * SK * DH * 2;
  const char* wg = (br ? wqB : wqA) + h * DH * 2;

  const unsigned vk0 = (16 * w + g) * 256 + 16 * (r ^ g);
  const unsigned vv0 = (16 * w + g) * 256 + 16 * (r ^ (2 * g));
  const unsigned vw0 = (16 * w + g) * (HD * 2) + 16 * (r ^ (2 * g));
  constexpr unsigned WSTEP = 4 * HD * 2 - 1024;
  const unsigned ldsw = lds0 + w * 4096;
#define DMA_K(slot, t) dma4(ldsw + (slot) * TILE_B, kg + (t) * TILE_B, vk0, vk0 ^ 64, vk0 ^ 128, vk0 ^ 192)
#define DMA_V(slot, t) dma4(ldsw + (slot) * TILE_B, vg + (t) * TILE_B, vv0, vv0 ^ 128, vv0, vv0 ^ 128)
#define DMA_W(slot, t) dma4(ldsw + (slot) * TILE_B, wg + (size_t)(t) * 64 * HD * 2, vw0, vw0 + WSTEP, (vw0 ^ 128) + 2 * WSTEP, (vw0 ^ 128) + 3 * WSTEP)

  const int q4 = (lane >> 2) & 3, p4 = lane & 3, fsw = 4 * (g & 1) + q4;
  bf16x8 qf[2][4], q9[4];
  {
    const unsigned vxo = (32 * w + r) * (DM * 2) + 16 * g, vx9 = (128 + r) * (DM * 2) + 16 * g;
    const unsigned rdw = (8 * g + q4) * 256 + 32 * fsw + 8 * p4;
    XSet xs[3];
    f32x4 acc[2][8], acc9[2];
#pragma unroll
    for (int i = 0; i < 8; ++i) { acc[0][i] = f32x4{0, 0, 0, 0}; acc[1][i] = f32x4{0, 0, 0, 0}; }
    acc9[0] = f32x4{0, 0, 0, 0}; acc9[1] = f32x4{0, 0, 0, 0};
    constexpr int WSLOT[3] = {3, 4, 2};
    x_issue(xs[0], xg, vxo, vx9);       DMA_W(3, 0);
    x_issue(xs[1], xg + 128, vxo, vx9); DMA_W(4, 1);
    x_issue(xs[2], xg + 256, vxo, vx9); DMA_W(2, 2);
    DMA_K(0, 0); DMA_V(1, 0);
#pragma unroll
    for (int i = 0; i < 8; ++i) {
      if (i == 0) WAIT_VM(28); else if (i <= 2) WAIT_VM(18); else if (i <= 6) WAIT_VM(10); else WAIT_VM(4);
      x_pin(xs[i % 3]);
      BARRIER();
      if (i >= 1 && i <= 5) { x_issue(xs[(i + 2) % 3], xg + 128 * (i + 2), vxo, vx9); DMA_W(WSLOT[(i + 2) % 3], i + 2); }
      if (i == 6) DMA_K(2, 1);
      if (i == 7) DMA_V(3, 1);
      lds_char_p sl = smem + WSLOT[i % 3] * TILE_B;
#pragma unroll
      for (int ks = 0; ks < 2; ++ks) {
#pragma unroll
        for (int dt = 0; dt < 8; ++dt) {
          bf16x8 a = tr_frag(sl + ((rdw ^ (32 * dt)) + 8192 * ks), 1024);
          acc[0][dt] = mfma32(a, xs[i % 3].f[0][ks], acc[0][dt]);
          acc[1][dt] = mfma32(a, xs[i % 3].f[1][ks], acc[1][dt]);
        }
#pragma unroll
        for (int e = 0; e < 2; ++e) {
          bf16x8 a = tr_frag(sl + ((rdw ^ (64 * w + 32 * e)) + 8192 * ks), 1024);
          acc9[e] = mfma32(a, xs[i % 3].f[2][ks], acc9[e]);
        }
      }
      SCHED_FENCE();
    }
#pragma unroll
    for (int s = 0; s < 4; ++s) { qf[0][s] = q_frag(acc[0][2 * s], acc[0][2 * s + 1]); qf[1][s] = q_frag(acc[1][2 * s], acc[1][2 * s + 1]); }
    bf16x8 mine = q_frag(acc9[0], acc9[1]);
    BARRIER();
    *(__attribute__((address_space(3))) bf16x8*)(smem + 4 * TILE_B + w * 1024 + lane * 16) = mine;
    BARRIER();
#pragma unroll
    for (int s = 0; s < 4; ++s) q9[s] = *(__attribute__((address_space(3))) bf16x8*)(smem + 4 * TILE_B + s * 1024 + lane * 16);
  }

  f32x4 o[2][8], o9[8];
#pragma unroll
  for (int i = 0; i < 8; ++i) { o[0][i] = f32x4{0, 0, 0, 0}; o[1][i] = f32x4{0, 0, 0, 0}; o9[i] = f32x4{0, 0, 0, 0}; }
  float nm[3] = {0.f, 0.f, 0.f}, ls[3] = {0.f, 0.f, 0.f};
  const unsigned rdk = r * 256 + 16 * (g ^ r), rdv = (4 * g + q4) * 256 + 32 * fsw + 8 * p4;
  int sk = 0;
#pragma unroll 1
  for (int t = 0; t < 8; ++t) {
    const int sv = sk == 4 ? 0 : sk + 1;
    if (t > 0) {
      if (t < 7) WAIT_VM(12); else WAIT_VM(4);
      BARRIER();
      if (t < 6) DMA_K(sk == 0 ? 4 : sk - 1, t + 2);
    }
    f32x4 s[2][4], s9;
    {
      lds_char_p sl = smem + sk * TILE_B;
      const f32x4 c0 = {nm[0], nm[0], nm[0], nm[0]}, c1 = {nm[1], nm[1], nm[1], nm[1]}, c9 = {nm[2], nm[2], nm[2], nm[2]};
#pragma unroll
      for (int kt = 0; kt < 4; ++kt) {
#pragma unroll
        for (int ks = 0; ks < 4; ++ks) {
          bf16x8 a = *(__attribute__((address_space(3))) bf16x8*)(sl + ((rdk ^ (64 * ks)) + 4096 * kt));
          s[0][kt] = mfma32(a, qf[0][ks], ks ? s[0][kt] : c0);
          s[1][kt] = mfma32(a, qf[1][ks], ks ? s[1][kt] : c1);
        }
      }
#pragma unroll
      for (int ks = 0; ks < 4; ++ks) {
        bf16x8 a = *(__attribute__((address_space(3))) bf16x8*)(sl + ((rdk ^ (64 * ks)) + 4096 * w));
        s9 = mfma32(a, q9[ks], ks ? s9 : c9);
      }
    }
    float mx[3];
#pragma unroll
    for (int qt = 0; qt < 2; ++qt) {
      float m = max3(s[qt][0][0], s[qt][0][1], s[qt][0][2]);
      m = max3(m, s[qt][0][3], s[qt][1][0]); m = max3(m, s[qt][1][1], s[qt][1][2]);
      m = max3(m, s[qt][1][3], s[qt][2][0]); m = max3(m, s[qt][2][1], s[qt][2][2]);
      m = max3(m, s[qt][2][3], s[qt][3][0]); m = max3(m, s[qt][3][1], s[qt][3][2]);
      mx[qt] = max3(m, s[qt][3][3], s[qt][3][3]);
    }
    mx[2] = max3(max3(s9[0], s9[1], s9[2]), s9[3], s9[3]);
    if (t == 0 || __any(max3(mx[0], mx[1], mx[2]) > RESCALE_THR)) {
      float d[3];
#pragma unroll
      for (int qt = 0; qt < 3; ++qt) {
        d[qt] = xg_max(mx[qt]);
        if (t > 0) d[qt] = fmaxf(d[qt], 0.f);
        nm[qt] -= d[qt];
      }
#pragma unroll
      for (int kt = 0; kt < 4; ++kt) { s[0][kt] -= d[0]; s[1][kt] -= d[1]; }
      s9 -= d[2];
      if (t > 0) {
        const float a0 = __builtin_amdgcn_exp2f(-d[0]), a1 = __builtin_amdgcn_exp2f(-d[1]), a2 = __builtin_amdgcn_exp2f(-d[2]);
        ls[0] *= a0; ls[1] *= a1; ls[2] *= a2;
#pragma unroll
        for (int dt = 0; dt < 8; ++dt) { o[0][dt] *= a0; o[1][dt] *= a1; o9[dt] *= a2; }
      }
    }
    bf16x8 p[2][2];
    s16x4 p9;
#pragma unroll
    for (int qt = 0; qt < 2; ++qt) {
#pragma unroll
      for (int kt = 0; kt < 4; ++kt) {
#pragma unroll
        for (int e = 0; e < 4; ++e) { s[qt][kt][e] = __builtin_amdgcn_exp2f(s[qt][kt][e]); ls[qt] += s[qt][kt][e]; }
      }
      p[qt][0] = pack8(s[qt][0], s[qt][1]); p[qt][1] = pack8(s[qt][2], s[qt][3]);
    }
#pragma unroll
    for (int e = 0; e < 4; ++e) { s9[e] = __builtin_amdgcn_exp2f(s9[e]); ls[2] += s9[e]; }
    { u32x2 u = {pk2(s9[0], s9[1]), pk2(s9[2], s9[3])}; p9 = __builtin_bit_cast(s16x4, u); }

    if (t < 6) WAIT_VM(12); else if (t == 6) WAIT_VM(8); else WAIT_VM(0);
    BARRIER();
    if (t == 0) DMA_K(4, 2);
    if (t < 6) DMA_V(sk, t + 2);
    {
      lds_char_p sl = smem + sv * TILE_B;
#pragma unroll
      for (int dt = 0; dt < 8; ++dt) {
#pragma unroll
        for (int ks = 0; ks < 2; ++ks) {
          bf16x8 a = tr_frag(sl + ((rdv ^ (32 * dt)) + 8192 * ks), 4096);
          o[0][dt] = mfma32(a, p[0][ks], o[0][dt]);
          o[1][dt] = mfma32(a, p[1][ks], o[1][dt]);
        }
        s16x4 a9 = tr_read(sl + ((rdv ^ (32 * dt)) + 4096 * w));
        o9[dt] = mfma16(a9, p9, o9[dt]);
      }
    }
    sk = sv == 4 ? 0 : sv + 1;
  }

  char* ob = ows + ((size_t)((br * NB + b) * NQT) * NH + h) * 4096 + lane * 16;
#pragma unroll
  for (int qt = 0; qt < 2; ++qt) {
    const float inv = 1.0f / xg_sum(ls[qt]);
    char* dst = ob + (size_t)(2 * w + qt) * NH * 4096;
#pragma unroll
    for (int s = 0; s < 4; ++s) *(bf16x8*)(dst + s * 1024) = pack8(o[qt][2 * s] * inv, o[qt][2 * s + 1] * inv);
  }
  {
    lds_char_p xo = smem + TILE_B;
    lds_char_p xst = smem + 3 * TILE_B;
    const float l9 = xg_sum(ls[2]);
#pragma unroll
    for (int dt = 0; dt < 8; ++dt) *(__attribute__((address_space(3))) f32x4*)(xo + (w * 8 + dt) * 1024 + lane * 16) = o9[dt];
    *(__attribute__((address_space(3))) f32x2*)(xst + w * 512 + lane * 8) = f32x2{nm[2], l9};
    BARRIER();
    f32x2 st[4];
#pragma unroll
    for (int j = 0; j < 4; ++j) st[j] = *(__attribute__((address_space(3))) f32x2*)(xst + j * 512 + lane * 8);
    const float nmin = fminf(fminf(st[0][0], st[1][0]), fminf(st[2][0], st[3][0]));
    float f[4], l = 0.f;
#pragma unroll
    for (int j = 0; j < 4; ++j) { f[j] = __builtin_amdgcn_exp2f(nmin - st[j][0]); l += f[j] * st[j][1]; }
    const float inv = 1.0f / l;
    f32x4 t0 = {0, 0, 0, 0}, t1 = {0, 0, 0, 0};
#pragma unroll
    for (int j = 0; j < 4; ++j) {
      t0 += *(__attribute__((address_space(3))) f32x4*)(xo + (j * 8 + 2 * w) * 1024 + lane * 16) * (f[j] * inv);
      t1 += *(__attribute__((address_space(3))) f32x4*)(xo + (j * 8 + 2 * w + 1) * 1024 + lane * 16) * (f[j] * inv);
    }
    *(bf16x8*)(ob + (size_t)8 * NH * 4096 + w * 1024) = pack8(t0, t1);
  }
#undef DMA_K
#undef DMA_V
#undef DMA_W
}

template <int NQ> struct OSet { bf16x8 f[NQ]; };
template <int NQ> DI void o_issue(OSet<NQ>& s, const char* ob, unsigned vo) {
#pragma unroll
  for (int i = 0; i < NQ; ++i) gload16<0>(s.f[i], ob + (size_t)i * NH * 4096, vo);
}
template <int NQ> DI void o_pin(OSet<NQ>& s) {
#pragma unroll
  for (int i = 0; i < NQ; ++i) asm volatile("" : "+v"(s.f[i]));
}
template <int N> DI void wait_vm() { asm volatile("s_waitcnt vmcnt(%0)" :: "i"(N) : "memory"); }

template <int NQ>
DI void oproj_body(lds_char_p smem, const char* ob, const char* wo, char* outp, int w, int tid) {
  const int lane = tid & 63, g = lane >> 4, q4 = (lane >> 2) & 3, p4 = lane & 3;
  const unsigned lds0 = (unsigned)(uintptr_t)smem + w * 20480;
  lds_char_p my = smem + w * 20480;
  const int drow = lane >> 3;
  const unsigned vw0 = drow * (DM * 2) + 16 * ((lane & 7) ^ (((drow >> 1) & 3) << 1));
  constexpr unsigned WSTEP = 8 * DM * 2 - 1024;
  const unsigned vo = lane * 16;
  const int rrow = 4 * g + q4;
  const unsigned rd = rrow * 128 + 32 * ((rrow >> 1) & 3) + 8 * p4;
  const char* wk = wo + (size_t)(16 * w) * 32 * DM * 2;
  const char* ok = ob + (16 * w) * 1024;
  constexpr int PER = NQ + 4;
  OSet<NQ> os[4];
  f32x4 acc[NQ][4];
#pragma unroll
  for (int i = 0; i < NQ; ++i)
#pragma unroll
    for (int n = 0; n < 4; ++n) acc[i][n] = f32x4{0, 0, 0, 0};
#pragma unroll
  for (int i = 0; i < 4; ++i) {
    dma4(lds0 + i * 4096, wk + (size_t)i * 32 * DM * 2, vw0, vw0 + WSTEP, vw0 + 2 * WSTEP, vw0 + 3 * WSTEP);
    o_issue<NQ>(os[i], ok + i * 1024, vo);
  }
#pragma unroll
  for (int i = 0; i < 16; ++i) {
    if (i <= 12) wait_vm<3 * PER>(); else if (i == 13) wait_vm<2 * PER>(); else if (i == 14) wait_vm<PER>(); else wait_vm<0>();
    o_pin<NQ>(os[i & 3]);
#pragma unroll
    for (int n = 0; n < 4; ++n) {
      bf16x8 a = tr_frag(my + ((rd ^ (32 * n)) + (i & 3) * 4096), 2048);
#pragma unroll
      for (int j = 0; j < NQ; ++j) acc[j][n] = mfma32(a, os[i & 3].f[j], acc[j][n]);
    }
    SCHED_FENCE();
    if (i + 4 < 16) {
      asm volatile("s_waitcnt lgkmcnt(0)" ::: "memory");
      dma4(lds0 + (i & 3) * 4096, wk + (size_t)(i + 4) * 32 * DM * 2, vw0, vw0 + WSTEP, vw0 + 2 * WSTEP, vw0 + 3 * WSTEP);
      o_issue<NQ>(os[i & 3], ok + (i + 4) * 1024, vo);
    }
    SCHED_FENCE();
  }
#pragma unroll
  for (int j = 0; j < NQ; ++j)
#pragma unroll
    for (int n = 0; n < 4; ++n) *(__attribute__((address_space(3))) f32x4*)(my + (j * 4 + n) * 1024 + lane * 16) = acc[j][n];
  BARRIER();
#pragma unroll
  for (int it = 0; it < 3; ++it) {
    const int tile = it * 8 + w;
    if (tile < NQ * 4) {
      f32x4 sum = {0, 0, 0, 0};
#pragma unroll
      for (int j = 0; j < 8; ++j) sum += *(__attribute__((address_space(3))) f32x4*)(smem + j * 20480 + tile * 1024 + lane * 16);
      const int qt = tile >> 2, n = tile & 3;
      u32x2 u = {pk2(sum[0], sum[1]), pk2(sum[2], sum[3])};
      *(u32x2*)(outp + (size_t)(16 * qt + (lane & 15)) * (2 * DM * 2) + (16 * n + 4 * g) * 2) = u;
    }
  }
}

__global__ __launch_bounds__(512, 2)
void oproj_kernel(const char* __restrict__ ows, const char* __restrict__ woA, const char* __restrict__ woB, char* __restrict__ out) {
  extern __shared__ __attribute__((aligned(16))) char smem_raw[];
  lds_char_p smem = (lds_char_p)smem_raw;
  const int tid = threadIdx.x;
  const int w = __builtin_amdgcn_readfirstlane(tid >> 6);
  const int bid = blockIdx.x, xcd = bid & 7, idx = bid >> 3;
  const int br = xcd >> 2, b = 2 * (xcd & 3) + (idx & 1), rg = (idx >> 1) & 1, cg = idx >> 2;
  const int qt0 = rg ? 5 : 0;
  const char* ob = ows + (size_t)((br * NB + b) * NQT + qt0) * NH * 4096;
  const char* wo = (br ? woB : woA) + cg * 128;
  char* outp = out + ((size_t)(b * SQ + 16 * qt0) * (2 * DM) + br * DM + cg * 64) * 2;
  if (rg == 0) oproj_body<5>(smem, ob, wo, outp, w, tid);
  else         oproj_body<4>(smem, ob, wo, outp, w, tid);
}

extern "C" void kernel_launch(void* const* d_in, const int* in_sizes, int n_in,
                              void* d_out, int out_size, void* d_ws, size_t ws_size,
                              hipStream_t stream) {
  (void)in_sizes; (void)n_in; (void)out_size; (void)ws_size;
  (void)hipFuncSetAttribute((const void*)qattn_kernel, hipFuncAttributeMaxDynamicSharedMemorySize, LDS1);
  (void)hipFuncSetAttribute((const void*)oproj_kernel, hipFuncAttributeMaxDynamicSharedMemorySize, LDS2);
  hipLaunchKernelGGL(qattn_kernel, dim3(2 * NB * NH), dim3(256), LDS1, stream,
                     (const char*)d_in[0], (const char*)d_in[1], (const char*)d_in[2], (const char*)d_in[3],
                     (const char*)d_in[5], (const char*)d_in[6], (const char*)d_in[7], (const char*)d_in[8], (char*)d_ws);
  hipLaunchKernelGGL(oproj_kernel, dim3(256), dim3(512), LDS2, stream,
                     (const char*)d_ws, (const char*)d_in[4], (const char*)d_in[9], (char*)d_out);
}
```

```cpp
#include <hip/hip_runtime.h>
#include <hip/hip_bf16.h>
#include <stdint.h>
#include <utility>

typedef short    s16x4  __attribute__((ext_vector_type(4)));
typedef short    s16x8  __attribute__((ext_vector_type(8)));
typedef __bf16   bf16x2 __attribute__((ext_vector_type(2)));
typedef __bf16   bf16x8 __attribute__((ext_vector_type(8)));
typedef float    f32x2  __attribute__((ext_vector_type(2)));
typedef float    f32x4  __attribute__((ext_vector_type(4)));
typedef unsigned u32x2  __attribute__((ext_vector_type(2)));
typedef unsigned u32x4  __attribute__((ext_vector_type(4)));
typedef __attribute__((address_space(3))) s16x4* lds_s16x4_p;
typedef __attribute__((address_space(3))) char* lds_char_p;

#define DI __device__ __forceinline__
#define W_NT false
#define X_NT false
#define O_NT 0
#define GRID1 512

constexpr int NB = 8, SQ = 144, DM = 512, NH = 32, SK = 512, DH = 128, HD = NH * DH;
constexpr int NQT = SQ / 16;
constexpr int TILE_B = 64 * 256;
constexpr int LDS1 = 5 * TILE_B;
constexpr int LDS2 = 8 * 20480;
constexpr float QSCALE = 0.08838834764831845f * 1.4426950408889634f;
constexpr float RESCALE_THR = 8.0f;

template <class F, int... Is> DI void static_for_impl(F&& f, std::integer_sequence<int, Is...>) { (f(std::integral_constant<int, Is>{}), ...); }
template <int N, class F> DI void static_for(F&& f) { static_for_impl(f, std::make_integer_sequence<int, N>{}); }
#define FOR_I(N) static_for<N>([&](auto ic_) __attribute__((always_inline)) { constexpr int i = decltype(ic_)::value;
#define END_FOR });

DI f32x4 mfma32(bf16x8 a, bf16x8 b, f32x4 c) { return __builtin_amdgcn_mfma_f32_16x16x32_bf16(a, b, c, 0, 0, 0); }
DI f32x4 mfma16(s16x4 a, s16x4 b, f32x4 c) { return __builtin_amdgcn_mfma_f32_16x16x16bf16_1k(a, b, c, 0, 0, 0); }
DI unsigned pk2(float lo, float hi) { f32x2 v = {lo, hi}; return __builtin_bit_cast(unsigned, __builtin_convertvector(v, bf16x2)); }
DI s16x4 tr_read(lds_char_p p) { return __builtin_amdgcn_ds_read_tr16_b64_v4i16((lds_s16x4_p)p); }
DI bf16x8 tr_frag(lds_char_p p, int second) {
  s16x4 lo = tr_read(p), hi = tr_read(p + second);
  return __builtin_bit_cast(bf16x8, __builtin_shufflevector(lo, hi, 0, 1, 2, 3, 4, 5, 6, 7));
}
DI bf16x8 pack8(f32x4 a, f32x4 b) {
  u32x4 w = {pk2(a[0], a[1]), pk2(a[2], a[3]), pk2(b[0], b[1]), pk2(b[2], b[3])};
  return __builtin_bit_cast(bf16x8, w);
}
DI float max3(float a, float b, float c) { return __builtin_fmaxf(__builtin_fmaxf(a, b), c); }
DI float xg_max(float x) {
  auto a = __builtin_amdgcn_permlane32_swap(__float_as_uint(x), __float_as_uint(x), false, false);
  x = fmaxf(__uint_as_float(a[0]), __uint_as_float(a[1]));
  auto b = __builtin_amdgcn_permlane16_swap(__float_as_uint(x), __float_as_uint(x), false, false);
  return fmaxf(__uint_as_float(b[0]), __uint_as_float(b[1]));
}
DI float xg_sum(float x) {
  auto a = __builtin_amdgcn_permlane32_swap(__float_as_uint(x), __float_as_uint(x), false, false);
  x = __uint_as_float(a[0]) + __uint_as_float(a[1]);
  auto b = __builtin_amdgcn_permlane16_swap(__float_as_uint(x), __float_as_uint(x), false, false);
  return __uint_as_float(b[0]) + __uint_as_float(b[1]);
}

#define DMA_HEAD "s_mov_b32 %0, m0\n\ts_mov_b32 m0, %1\n\ts_nop 0\n\t"
#define DMA_TAIL "s_mov_b32 m0, %0"
DI void dma4(unsigned lds, const char* base, unsigned v0, unsigned v1, unsigned v2, unsigned v3) {
  unsigned keep;
  lds = __builtin_amdgcn_readfirstlane(lds);
  asm volatile(DMA_HEAD
      "global_load_lds_dwordx4 %3, %2\n\t"
      "global_load_lds_dwordx4 %4, %2 offset:1024\n\t"
      "global_load_lds_dwordx4 %5, %2 offset:2048\n\t"
      "global_load_lds_dwordx4 %6, %2 offset:3072\n\t" DMA_TAIL
      : "=&s"(keep) : "s"(lds), "s"(base), "v"(v0), "v"(v1), "v"(v2), "v"(v3) : "memory");
}
template <int OFF, bool NT> DI void dma1(unsigned lds, const char* base, unsigned v) {
  lds = __builtin_amdgcn_readfirstlane(lds);
  if constexpr (NT) asm volatile("s_mov_b32 m0, %0\n\ts_nop 0\n\tglobal_load_lds_dwordx4 %2, %1 offset:%3 nt" :: "s"(lds), "s"(base), "v"(v), "i"(OFF) : "memory");
  else              asm volatile("s_mov_b32 m0, %0\n\ts_nop 0\n\tglobal_load_lds_dwordx4 %2, %1 offset:%3" :: "s"(lds), "s"(base), "v"(v), "i"(OFF) : "memory");
}
template <int OFF> DI void gload16(bf16x8& dst, const char* base, unsigned voff) {
  asm volatile("global_load_dwordx4 %0, %1, %2 offset:%3" : "=v"(dst) : "v"(voff), "s"(base), "i"(OFF) : "memory");
}
#define WAIT_VM(n) asm volatile("s_waitcnt vmcnt(" #n ")" ::: "memory")
#define MEM_FENCE() asm volatile("" ::: "memory")
#define WAIT_LGKM(n) do { MEM_FENCE(); __builtin_amdgcn_s_waitcnt(0xC07F | ((n) << 8)); MEM_FENCE(); } while (0)
#define BARRIER() do { WAIT_LGKM(0); __builtin_amdgcn_s_barrier(); MEM_FENCE(); } while (0)
#define SCHED_FENCE() do { MEM_FENCE(); __builtin_amdgcn_sched_barrier(0); } while (0)

#define STAMP(i)
#define TICK(i)
#define dg_zero7
#define DIAG_DECL(n)
#define DIAG_FLUSH(n, ptr, slot)

DI bf16x8 q_frag(f32x4 t0, f32x4 t1) {
  unsigned a0 = pk2(t0[0] * QSCALE, t0[1] * QSCALE), a1 = pk2(t0[2] * QSCALE, t0[3] * QSCALE);
  unsigned b0 = pk2(t1[0] * QSCALE, t1[1] * QSCALE), b1 = pk2(t1[2] * QSCALE, t1[3] * QSCALE);
  auto p0 = __builtin_amdgcn_permlane32_swap(a0, b0, false, false);
  auto p1 = __builtin_amdgcn_permlane32_swap(a1, b1, false, false);
  auto r0 = __builtin_amdgcn_permlane16_swap(p0[0], p0[1], false, false);
  auto r1 = __builtin_amdgcn_permlane16_swap(p1[0], p1[1], false, false);
  u32x4 w = {r0[0], r1[0], r0[1], r1[1]};
  return __builtin_bit_cast(bf16x8, w);
}

DI void ostore(char* p, bf16x8 v) {
  *(bf16x8*)p = v;
}
#define LDS_RD(T, p) (*(__attribute__((address_space(3))) T*)(p))
template <int N> DI void wait_vm() { asm volatile("s_waitcnt vmcnt(%0)" :: "i"(N) : "memory"); }

struct AttnState {
  bf16x8 qf[2][4], q9[4];
  f32x4 o[2][8], o9[8];
  f32x4 c[3];
  float ls[3];
  f32x4 sE[2][2], sO[2][2], s9;
  u32x4 pE[2];
  bf16x8 kc[4];
  unsigned rdk, rdv, vk0, vv0, ldsw;
  const char *kg, *vg;
  lds_char_p smem;
  int w;
};
#define EXP2X(v, e, dst, l) do { const float x0_ = __builtin_amdgcn_exp2f(v[e]), x1_ = __builtin_amdgcn_exp2f(v[(e) + 1]); \
    dst = pk2(x0_, x1_); l += x0_; l += x1_; asm volatile("" : "+v"(l), "+v"(dst)); } while (0)
#define MAX2X(m, v, e) do { m = max3(m, v[e], v[(e) + 1]); asm volatile("" : "+v"(m)); } while (0)
#define BF8(x) __builtin_bit_cast(bf16x8, x)
#define K_RD(base, kt, s) LDS_RD(bf16x8, (base) + 4096 * (kt) + 512 * (s))
#define V_RD(dt, ks) tr_frag((((dt) & 1) ? vo : ve) + 512 * ((dt) >> 1) + 8192 * (ks), 4096)
#define QK2(dst, j, a, i) do { dst[0][j] = mfma32(a, A.qf[0][i], (i) ? dst[0][j] : A.c[0]); dst[1][j] = mfma32(a, A.qf[1][i], (i) ? dst[1][j] : A.c[1]); } while (0)
template <int I> DI void dma_k(const AttnState& A, int slot, int t) {
  dma1<128 * (I & 1) + 2048 * (I >> 1), true>(A.ldsw + slot * TILE_B + 896 * (I & 1), A.kg + t * TILE_B, I < 2 ? A.vk0 : (A.vk0 ^ 32));
}
template <int I> DI void dma_v(const AttnState& A, int slot, int t) {
  dma1<128 * (I & 1) + 2048 * (I >> 1), true>(A.ldsw + slot * TILE_B + 896 * (I & 1), A.vg + t * TILE_B, A.vv0);
}
DI void dma_k4(const AttnState& A, int slot, int t) { dma_k<0>(A, slot, t); dma_k<1>(A, slot, t); dma_k<2>(A, slot, t); dma_k<3>(A, slot, t); }
DI void dma_v4(const AttnState& A, int slot, int t) { dma_v<0>(A, slot, t); dma_v<1>(A, slot, t); dma_v<2>(A, slot, t); dma_v<3>(A, slot, t); }
template <int I> DI void dma_kv(const AttnState& A, int slotk, int slotv, int t) {
  if constexpr (I < 4) dma_k<I>(A, slotk, t); else dma_v<I - 4>(A, slotv, t);
}
DI void rescale(f32x4& c, float& l, f32x4* o, f32x4& s0, f32x4& s1) {
  const float mx = max3(max3(max3(max3(0.f, s0[0], s0[1]), s0[2], s0[3]), s1[0], s1[1]), s1[2], s1[3]);
  const float d = xg_max(mx), a = __builtin_amdgcn_exp2f(-d);
  c -= d; s0 -= d; s1 -= d; l *= a;
#pragma unroll
  for (int dt = 0; dt < 8; ++dt) o[dt] *= a;
}
DI void attn_x2a(AttnState& A, lds_char_p kn, bf16x8* k2) {
#pragma unroll
  for (int i = 0; i < 4; ++i) {
    QK2(A.sO, 0, k2[i], i);
    A.kc[i] = K_RD(kn, 3, i);
    EXP2X(A.sE[0][i >> 1], 2 * (i & 1), A.pE[0][i], A.ls[0]);
    SCHED_FENCE();
  }
}
template <int MODE> DI void attn_step(AttnState& A, int t, int sk) {
  constexpr bool LAST = MODE == 2;
  const int sv = sk == 4 ? 0 : sk + 1, sn = sv == 4 ? 0 : sv + 1, sp = sk == 0 ? 4 : sk - 1;
  if constexpr (LAST) wait_vm<0>(); else wait_vm<4>();
  BARRIER();
  lds_char_p ve = A.smem + (A.rdv + sv * TILE_B), vo = A.smem + ((A.rdv ^ 32) + sv * TILE_B);
  lds_char_p kn = A.smem + (A.rdk + sn * TILE_B);
  bf16x8 va[8], vb[8], k0[4], k1[4], k2[4], k9[4];
  s16x4 v9[8];
  u32x4 pO[2];
  u32x2 p9;
  float mx;
  bool grow;
  FOR_I(4)
    if constexpr (i < 3) { va[2 * i] = V_RD(2 * i, 0); va[2 * i + 1] = V_RD(2 * i + 1, 0); }
    QK2(A.sO, 1, A.kc[i], i);
    EXP2X(A.sE[1][i >> 1], 2 * (i & 1), A.pE[1][i], A.ls[1]);
    SCHED_FENCE();
  END_FOR
  lds_char_p ve9 = ve + 4096 * A.w, vo9 = vo + 4096 * A.w;
  FOR_I(8)
    A.o[0][i] = mfma32(va[i], BF8(A.pE[0]), A.o[0][i]);
    if constexpr (i < 2) va[6 + i] = V_RD(6 + i, 0);
    else if constexpr (i < 6) { v9[2 * (i - 2)] = tr_read(ve9 + 512 * (i - 2)); v9[2 * (i - 2) + 1] = tr_read(vo9 + 512 * (i - 2)); }
    else if constexpr (!LAST) k0[i - 6] = K_RD(kn, 0, i - 6);
    A.o[1][i] = mfma32(va[i], BF8(A.pE[1]), A.o[1][i]);
    if constexpr (MODE == 0) dma_kv<i>(A, sp, sk, t + 2);
    if constexpr (i == 0) { mx = 0.f; EXP2X(A.s9, 0, p9[0], A.ls[2]); }
    if constexpr (i == 1) EXP2X(A.s9, 2, p9[1], A.ls[2]);
    if constexpr (i >= 2) MAX2X(mx, A.sO[(i >> 1) & 1][(i - 2) >> 2], 2 * (i & 1));
    SCHED_FENCE();
  END_FOR
  FOR_I(4)
    A.o9[2 * i] = mfma16(v9[2 * i], __builtin_bit_cast(s16x4, p9), A.o9[2 * i]);
    if constexpr (LAST) { vb[2 * i] = V_RD(2 * i, 1); vb[2 * i + 1] = V_RD(2 * i + 1, 1); }
    else if constexpr (i < 2) k0[2 + i] = K_RD(kn, 0, 2 + i);
    else k1[i - 2] = K_RD(kn, 1, i - 2);
    A.o9[2 * i + 1] = mfma16(v9[2 * i + 1], __builtin_bit_cast(s16x4, p9), A.o9[2 * i + 1]);
    if constexpr (i < 2) MAX2X(mx, A.sO[0][1], 2 * i);
    if constexpr (i == 2) grow = __any(mx > RESCALE_THR);
    SCHED_FENCE();
  END_FOR
  if (__builtin_expect(grow, 0)) {
    rescale(A.c[0], A.ls[0], A.o[0], A.sO[0][0], A.sO[0][1]);
    rescale(A.c[1], A.ls[1], A.o[1], A.sO[1][0], A.sO[1][1]);
  }
  SCHED_FENCE();
  lds_char_p kn9 = kn + 4096 * A.w;
  FOR_I(8)
    if constexpr (!LAST) {
      if constexpr (i < 4) QK2(A.sE, 0, k0[i], i); else QK2(A.sE, 1, k1[i - 4], i - 4);
      if constexpr (i < 2) k1[2 + i] = K_RD(kn, 1, 2 + i);
      else if constexpr (i < 6) k9[i - 2] = K_RD(kn9, 0, i - 2);
      else vb[i - 6] = V_RD(i - 6, 1);
    }
    EXP2X(A.sO[i >> 2][(i >> 1) & 1], 2 * (i & 1), pO[i >> 2][i & 3], A.ls[i >> 2]);
    SCHED_FENCE();
  END_FOR
  if constexpr (!LAST) {
    FOR_I(2)
      A.s9 = mfma32(k9[2 * i], A.q9[2 * i], i ? A.s9 : A.c[2]);
      vb[2 + 2 * i] = V_RD(2 + 2 * i, 1);
      A.s9 = mfma32(k9[2 * i + 1], A.q9[2 * i + 1], A.s9);
      vb[3 + 2 * i] = V_RD(3 + 2 * i, 1);
      SCHED_FENCE();
    END_FOR
  }
  FOR_I(8)
    A.o[0][i] = mfma32(vb[i], BF8(pO[0]), A.o[0][i]);
    if constexpr (!LAST) {
      if constexpr (i < 2) vb[6 + i] = V_RD(6 + i, 1);
      else if constexpr (i < 6) k2[i - 2] = K_RD(kn, 2, i - 2);
    }
    A.o[1][i] = mfma32(vb[i], BF8(pO[1]), A.o[1][i]);
    if constexpr (!LAST) {
      if constexpr (i == 0) mx = 0.f;
      if constexpr (i < 4) { MAX2X(mx, A.sE[i & 1][i >> 1], 0); MAX2X(mx, A.sE[i & 1][i >> 1], 2); }
      if constexpr (i == 4 || i == 5) MAX2X(mx, A.s9, 2 * (i - 4));
      if constexpr (i == 6) grow = __any(mx > RESCALE_THR);
    }
    SCHED_FENCE();
  END_FOR
  if constexpr (!LAST) {
    if (__builtin_expect(grow, 0)) {
      f32x4 none = {0, 0, 0, 0};
      rescale(A.c[0], A.ls[0], A.o[0], A.sE[0][0], A.sE[0][1]);
      rescale(A.c[1], A.ls[1], A.o[1], A.sE[1][0], A.sE[1][1]);
      rescale(A.c[2], A.ls[2], A.o9, A.s9, none);
    }
    SCHED_FENCE();
    attn_x2a(A, kn, k2);
  }
}

__global__ __launch_bounds__(256, 2)
void qattn_kernel(const char* __restrict__ xA, const char* __restrict__ kA, const char* __restrict__ vA, const char* __restrict__ wqA,
                  const char* __restrict__ xB, const char* __restrict__ kB, const char* __restrict__ vB, const char* __restrict__ wqB,
                  char* __restrict__ ows) {
  extern __shared__ __attribute__((aligned(16))) char smem_raw[];
  lds_char_p smem = (lds_char_p)smem_raw;
  const unsigned lds0 = (unsigned)(uintptr_t)smem;
  const int tid = threadIdx.x, lane = tid & 63, r = lane & 15, g = lane >> 4;
  const int w = __builtin_amdgcn_readfirstlane(tid >> 6);
  const int bid = blockIdx.x, xcd = bid & 7, idx = bid >> 3;
  const int br = xcd >> 2, h = 8 * (xcd & 3) + (idx & 7), b = idx >> 3;
  const bool lead = bid < 256;
  const char* xg = (br ? xB : xA) + (size_t)b * SQ * DM * 2;
  const char* wg = (br ? wqB : wqA) + h * DH * 2;
  const int q4 = (lane >> 2) & 3, p4 = lane & 3, r8 = (lane >> 2) & 7;

  AttnState A;
  A.smem = smem; A.w = w;
  A.kg = (br ? kB : kA) + (size_t)(b * NH + h) * SK * DH * 2;
  A.vg = (br ? vB : vA) + (size_t)(b * NH + h) * SK * DH * 2;
  A.ldsw = lds0 + w * 4096;
  A.vk0 = (16 * w + r8) * 256 + 64 * (lane >> 5) + 16 * p4;
  A.vv0 = A.vk0 ^ (32 * (g & 1));
  DIAG_DECL(6);
  STAMP(0);
  {
    constexpr int XOFF = 2 * TILE_B, WOFF = XOFF + 4 * 6144;
    const unsigned vw0 = (8 * w + r8) * (HD * 2) + 64 * (lane >> 5) + 16 * (p4 ^ (2 * (w & 1)));
#define WPIECE(s) ((((s) + 2) % 3) * 8192)
#define DMA_W(s, i) dma1<128 * (i), W_NT>(lds0 + WOFF + WPIECE(s) + w * 2048 + 896 * (i), wg + (size_t)(s) * 32 * HD * 2, vw0)
    const int xr = lane >> 3;
    const unsigned vx0 = xr * (DM * 2) + 16 * ((lane & 7) ^ xr);
    const unsigned ldsx = lds0 + XOFF + w * 6144;
    const char* xown = xg + 32 * w * DM * 2;
#define DMA_X(c, i) dma1<0, X_NT>(ldsx + 1024 * (i), ((i) < 4 ? xown + 8 * (i) * DM * 2 : xg + (128 + 8 * ((i) - 4)) * DM * 2) + 128 * (c), vx0)
    unsigned xrd0 = XOFF + w * 6144 + r * 128 + 16 * (g ^ (r & 7)), xrd1 = xrd0 ^ 64;
#define X_RD(set, i) xf[set][(i) >> 1][(i) & 1] = LDS_RD(bf16x8, smem + ((((i) & 1) ? xrd1 : xrd0) + 2048 * ((i) >> 1)))
    unsigned rw[4];
    rw[0] = WOFF + 2048 * g + 64 * q4 + 32 * (g & 1) + 8 * p4; rw[1] = rw[0] ^ 32; rw[2] = rw[0] + 512 * w; rw[3] = rw[1] + 512 * w;
    asm volatile("" : "+v"(rw[0]), "+v"(rw[1]), "+v"(rw[2]), "+v"(rw[3]));
#define W_RD(set, s, i) wf[set][i] = tr_frag(smem + (rw[((i) & 1) + 2 * ((i) >> 3)] + 512 * (((i) >> 1) & 3) + WPIECE(s)), 256)
    bf16x8 xf[2][3][2], wf[2][10];
    f32x4 acc[2][8], acc9[2];
#pragma unroll
    for (int i = 0; i < 8; ++i) { acc[0][i] = f32x4{0, 0, 0, 0}; acc[1][i] = f32x4{0, 0, 0, 0}; }
    acc9[0] = f32x4{0, 0, 0, 0}; acc9[1] = f32x4{0, 0, 0, 0};

    if (lead) __builtin_amdgcn_s_setprio(1);
    DMA_X(0, 0); DMA_X(0, 1); DMA_X(0, 2); DMA_X(0, 3); DMA_X(0, 4); DMA_X(0, 5);
    DMA_W(0, 0); DMA_W(0, 1); DMA_W(1, 0); DMA_W(1, 1); DMA_W(2, 0); DMA_W(2, 1);
    if (!lead) { dma_k4(A, 0, 0); dma_v4(A, 1, 0); wait_vm<12>(); } else wait_vm<4>();
    BARRIER();
    STAMP(1);
#pragma unroll
    for (int i = 0; i < 6; ++i) X_RD(0, i);
#pragma unroll
    for (int i = 0; i < 10; ++i) W_RD(0, 0, i);
    WAIT_LGKM(0);
    DMA_X(1, 0); DMA_X(1, 1); DMA_X(1, 2); DMA_X(1, 3); DMA_X(1, 4); DMA_X(1, 5);
    static_for<16>([&](auto sc_) __attribute__((always_inline)) {
      constexpr int s = decltype(sc_)::value;
      constexpr int cs = s & 1, ns = cs ^ 1, xs = (s >> 1) & 1;
      constexpr bool xodd = (s & 1) && s < 15, xdma = (s & 1) && s <= 11;
      TICK(s == 0 ? 7 : 0);
      if constexpr (s == 0) { if (lead) wait_vm<8>(); else wait_vm<16>(); }
      else if constexpr (s <= 12) { if constexpr (s & 1) wait_vm<2>(); else wait_vm<8>(); }
      else if constexpr (s == 13) { if (lead) wait_vm<10>(); else wait_vm<2>(); }
      else if constexpr (s == 14) { if (lead) wait_vm<8>(); else wait_vm<0>(); }
      TICK(1);
      if constexpr (s <= 14) BARRIER();
      TICK(2);
      if constexpr (s == 8) STAMP(2);
      SCHED_FENCE();
      FOR_I(9)
        if constexpr (i < 8) acc[0][i] = mfma32(wf[cs][i], xf[xs][0][cs], acc[0][i]);
        else acc9[0] = mfma32(wf[cs][8], xf[xs][2][cs], acc9[0]);
        if constexpr (xodd) {
          if constexpr (i < 2) { X_RD(xs ^ 1, 3 * i); X_RD(xs ^ 1, 3 * i + 1); }
          else if constexpr (i < 7) W_RD(ns, s + 1, 2 * (i - 2));
        } else if constexpr (s < 15 && i < 7) W_RD(ns, s + 1, i);
        SCHED_FENCE();
        if constexpr (i < 8) acc[1][i] = mfma32(wf[cs][i], xf[xs][1][cs], acc[1][i]);
        else acc9[1] = mfma32(wf[cs][9], xf[xs][2][cs], acc9[1]);
        if constexpr (xodd) {
          if constexpr (i < 2) X_RD(xs ^ 1, 3 * i + 2);
          else if constexpr (i < 7) W_RD(ns, s + 1, 2 * (i - 2) + 1);
        } else if constexpr (s < 15 && i < 3) W_RD(ns, s + 1, 7 + i);
        if constexpr (s <= 12 && i < 2) DMA_W(s + 3, i);
        if constexpr (xdma && i == 3) WAIT_LGKM(8);
        if constexpr (xdma && i >= 3) DMA_X((s + 3) >> 1, i - 3);
        if constexpr (s == 12 && i == 2) { if (lead) dma_k4(A, 0, 0); }
        if constexpr (s == 12 && i == 5) { if (lead) dma_v4(A, 1, 0); }
        if constexpr (s == 14 && i < 8) dma_kv<i>(A, 2, 3, 1);
        SCHED_FENCE();
      END_FOR
    });
    LDS_RD(bf16x8, smem + 4 * TILE_B + w * 1024 + lane * 16) = q_frag(acc9[0], acc9[1]);
#pragma unroll
    for (int s = 0; s < 4; ++s) { A.qf[0][s] = q_frag(acc[0][2 * s], acc[0][2 * s + 1]); A.qf[1][s] = q_frag(acc[1][2 * s], acc[1][2 * s + 1]); }
    wait_vm<12>();
    BARRIER();
#pragma unroll
    for (int s = 0; s < 4; ++s) A.q9[s] = LDS_RD(bf16x8, smem + 4 * TILE_B + s * 1024 + lane * 16);
    if (lead) __builtin_amdgcn_s_setprio(0);
#undef WPIECE
#undef DMA_W
#undef DMA_X
#undef X_RD
#undef W_RD
  }

  STAMP(3);
#pragma unroll
  for (int i = 0; i < 8; ++i) { A.o[0][i] = f32x4{0, 0, 0, 0}; A.o[1][i] = f32x4{0, 0, 0, 0}; A.o9[i] = f32x4{0, 0, 0, 0}; }
  A.rdk = 2048 * (r >> 3) + 64 * (r & 7) + 16 * (g ^ (2 * (r >> 3)));
  A.rdv = 2048 * (g >> 1) + 64 * (4 * (g & 1) + q4) + 32 * (g & 1) + 8 * p4;
  {
    lds_char_p kn = smem + A.rdk;
    bf16x8 k0[4], k1[4], k2[4], k9[4];
#pragma unroll
    for (int i = 0; i < 3; ++i) { A.c[i] = f32x4{0, 0, 0, 0}; A.ls[i] = 0.f; }
#pragma unroll
    for (int i = 0; i < 4; ++i) { k0[i] = K_RD(kn, 0, i); k1[i] = K_RD(kn, 1, i); k9[i] = K_RD(kn + 4096 * w, 0, i); k2[i] = K_RD(kn, 2, i); }
#pragma unroll
    for (int i = 0; i < 4; ++i) { QK2(A.sE, 0, k0[i], i); QK2(A.sE, 1, k1[i], i); A.s9 = mfma32(k9[i], A.q9[i], i ? A.s9 : A.c[2]); }
#pragma unroll
    for (int qt = 0; qt < 2; ++qt) {
      float m = max3(A.sE[qt][0][0], A.sE[qt][0][1], A.sE[qt][0][2]);
      m = max3(m, A.sE[qt][0][3], A.sE[qt][1][0]); m = max3(m, A.sE[qt][1][1], A.sE[qt][1][2]);
      m = xg_max(fmaxf(m, A.sE[qt][1][3]));
      A.c[qt] -= m; A.sE[qt][0] -= m; A.sE[qt][1] -= m;
    }
    const float m = xg_max(fmaxf(max3(A.s9[0], A.s9[1], A.s9[2]), A.s9[3]));
    A.c[2] -= m; A.s9 -= m;
    SCHED_FENCE();
    attn_x2a(A, kn, k2);
  }
  TICK(7);
  int sk = 0;
#pragma unroll 1
  for (int t = 0; t < 6; ++t) {
    asm volatile("" : "+v"(A.vk0), "+v"(A.rdv));
    attn_step<0>(A, t, sk);
    sk = sk >= 3 ? sk - 3 : sk + 2;
    if (t == 0) TICK(3); else TICK(4);
  }
  attn_step<1>(A, 6, 2);
  TICK(5);
  attn_step<2>(A, 7, 4);
  TICK(6);

  STAMP(4);
  int tz = threadIdx.x;
  asm volatile("" : "+v"(tz));
  const int lane2 = tz & 63;
#define lane lane2
  char* ob = ows + ((size_t)((br * NB + b) * NQT) * NH + h) * 4096 + lane * 16;
  lds_char_p xo = smem + TILE_B;
  lds_char_p xst = smem + 3 * TILE_B;
  {
    const float l9 = xg_sum(A.ls[2]);
#pragma unroll
    for (int dt = 0; dt < 8; ++dt) LDS_RD(f32x4, xo + (w * 8 + dt) * 1024 + lane * 16) = A.o9[dt];
    LDS_RD(f32x2, xst + w * 512 + lane * 8) = f32x2{A.c[2][0], l9};
  }
#pragma unroll
  for (int qt = 0; qt < 2; ++qt) {
    const float inv = 1.0f / xg_sum(A.ls[qt]);
    char* dst = ob + (size_t)(2 * w + qt) * NH * 4096;
#pragma unroll
    for (int s = 0; s < 4; ++s) ostore(dst + s * 1024, pack8(A.o[qt][2 * s] * inv, A.o[qt][2 * s + 1] * inv));
  }
  {
    BARRIER();
    f32x2 st[4];
#pragma unroll
    for (int j = 0; j < 4; ++j) st[j] = LDS_RD(f32x2, xst + j * 512 + lane * 8);
    const float nmin = fminf(fminf(st[0][0], st[1][0]), fminf(st[2][0], st[3][0]));
    float f[4], l = 0.f;
#pragma unroll
    for (int j = 0; j < 4; ++j) { f[j] = __builtin_amdgcn_exp2f(nmin - st[j][0]); l += f[j] * st[j][1]; }
    const float inv = 1.0f / l;
    f32x4 t0 = {0, 0, 0, 0}, t1 = {0, 0, 0, 0};
#pragma unroll
    for (int j = 0; j < 4; ++j) {
      t0 += LDS_RD(f32x4, xo + (j * 8 + 2 * w) * 1024 + lane * 16) * (f[j] * inv);
      t1 += LDS_RD(f32x4, xo + (j * 8 + 2 * w + 1) * 1024 + lane * 16) * (f[j] * inv);
    }
    ostore(ob + (size_t)8 * NH * 4096 + w * 1024, pack8(t0, t1));
  }
#undef lane
  STAMP(5);
}

template <int NQ> struct OSet { bf16x8 f[NQ]; };
template <int NQ> DI void o_issue(OSet<NQ>& s, const char* ob, unsigned vo) {
#pragma unroll
  for (int i = 0; i < NQ; ++i) gload16<0>(s.f[i], ob + (size_t)i * NH * 4096, vo);
}
template <int NQ> DI void o_pin(OSet<NQ>& s) {
#pragma unroll
  for (int i = 0; i < NQ; ++i) asm volatile("" : "+v"(s.f[i]));
}

template <int NQ>
DI void oproj_body(lds_char_p smem, const char* ob, const char* wo, char* outp, int w, int tid, const char* dbg) {
  DIAG_DECL(6);
  STAMP(0);
  const int lane = tid & 63, g = lane >> 4, q4 = (lane >> 2) & 3, p4 = lane & 3;
  const unsigned lds0 = (unsigned)(uintptr_t)smem + w * 20480;
  lds_char_p my = smem + w * 20480;
  const int drow = lane >> 3;
  const unsigned vw0 = drow * (DM * 2) + 16 * ((lane & 7) ^ (((drow >> 1) & 3) << 1));
  constexpr unsigned WSTEP = 8 * DM * 2 - 1024;
  const unsigned vo = lane * 16;
  const int rrow = 4 * g + q4;
  const unsigned rd = rrow * 128 + 32 * ((rrow >> 1) & 3) + 8 * p4;
  const char* wk = wo + (size_t)(16 * w) * 32 * DM * 2;
  const char* ok = ob + (16 * w) * 1024;
  constexpr int PER = NQ + 4;
  OSet<NQ> os[4];
  f32x4 acc[NQ][4];
#pragma unroll
  for (int i = 0; i < NQ; ++i)
#pragma unroll
    for (int n = 0; n < 4; ++n) acc[i][n] = f32x4{0, 0, 0, 0};
#pragma unroll
  for (int i = 0; i < 4; ++i) {
    dma4(lds0 + i * 4096, wk + (size_t)i * 32 * DM * 2, vw0, vw0 + WSTEP, vw0 + 2 * WSTEP, vw0 + 3 * WSTEP);
    o_issue<NQ>(os[i], ok + i * 1024, vo);
  }
#pragma unroll
  for (int i = 0; i < 16; ++i) {
    if (i <= 12) wait_vm<3 * PER>(); else if (i == 13) wait_vm<2 * PER>(); else if (i == 14) wait_vm<PER>(); else wait_vm<0>();
    o_pin<NQ>(os[i & 3]);
    if (i == 0) STAMP(1);
    if (i == 4) STAMP(2);
    if (i == 10) STAMP(3);
#pragma unroll
    for (int n = 0; n < 4; ++n) {
      bf16x8 a = tr_frag(my + ((rd ^ (32 * n)) + (i & 3) * 4096), 2048);
#pragma unroll
      for (int j = 0; j < NQ; ++j) acc[j][n] = mfma32(a, os[i & 3].f[j], acc[j][n]);
    }
    SCHED_FENCE();
    if (i + 4 < 16) {
      WAIT_LGKM(0);
      dma4(lds0 + (i & 3) * 4096, wk + (size_t)(i + 4) * 32 * DM * 2, vw0, vw0 + WSTEP, vw0 + 2 * WSTEP, vw0 + 3 * WSTEP);
      o_issue<NQ>(os[i & 3], ok + (i + 4) * 1024, vo);
    }
    SCHED_FENCE();
  }
  STAMP(4);
#pragma unroll
  for (int j = 0; j < NQ; ++j)
#pragma unroll
    for (int n = 0; n < 4; ++n) *(__attribute__((address_space(3))) f32x4*)(my + (j * 4 + n) * 1024 + lane * 16) = acc[j][n];
  BARRIER();
#pragma unroll
  for (int it = 0; it < 3; ++it) {
    const int tile = it * 8 + w;
    if (tile < NQ * 4) {
      f32x4 sum = {0, 0, 0, 0};
#pragma unroll
      for (int j = 0; j < 8; ++j) sum += *(__attribute__((address_space(3))) f32x4*)(smem + j * 20480 + tile * 1024 + lane * 16);
      const int qt = tile >> 2, n = tile & 3;
      u32x2 u = {pk2(sum[0], sum[1]), pk2(sum[2], sum[3])};
      *(u32x2*)(outp + (size_t)(16 * qt + (lane & 15)) * (2 * DM * 2) + (16 * n + 4 * g) * 2) = u;
    }
  }
  STAMP(5);
}

__global__ __launch_bounds__(512, 2)
void oproj_kernel(const char* __restrict__ ows, const char* __restrict__ woA, const char* __restrict__ woB, char* __restrict__ out) {
  extern __shared__ __attribute__((aligned(16))) char smem_raw[];
  lds_char_p smem = (lds_char_p)smem_raw;
  const int tid = threadIdx.x;
  const int w = __builtin_amdgcn_readfirstlane(tid >> 6);
  const int bid = blockIdx.x, xcd = bid & 7, idx = bid >> 3;
  const int br = xcd >> 2, b = 2 * (xcd & 3) + (idx & 1), rg = (idx >> 1) & 1, cg = idx >> 2;
  const int qt0 = rg ? 5 : 0;
  const char* ob = ows + (size_t)((br * NB + b) * NQT + qt0) * NH * 4096;
  const char* wo = (br ? woB : woA) + cg * 128;
  char* outp = out + ((size_t)(b * SQ + 16 * qt0) * (2 * DM) + br * DM + cg * 64) * 2;
  if (rg == 0) oproj_body<5>(smem, ob, wo, outp, w, tid, ows);
  else         oproj_body<4>(smem, ob, wo, outp, w, tid, ows);
}

extern "C" void kernel_launch(void* const* d_in, const int* in_sizes, int n_in,
                              void* d_out, int out_size, void* d_ws, size_t ws_size,
                              hipStream_t stream) {
  (void)in_sizes; (void)n_in; (void)out_size; (void)ws_size;
  (void)hipFuncSetAttribute((const void*)qattn_kernel, hipFuncAttributeMaxDynamicSharedMemorySize, LDS1);
  (void)hipFuncSetAttribute((const void*)oproj_kernel, hipFuncAttributeMaxDynamicSharedMemorySize, LDS2);
  hipLaunchKernelGGL(qattn_kernel, dim3(GRID1), dim3(256), LDS1, stream,
                     (const char*)d_in[0], (const char*)d_in[1], (const char*)d_in[2], (const char*)d_in[3],
                     (const char*)d_in[5], (const char*)d_in[6], (const char*)d_in[7], (const char*)d_in[8], (char*)d_ws);
  hipLaunchKernelGGL(oproj_kernel, dim3(256), dim3(512), LDS2, stream,
                     (const char*)d_ws, (const char*)d_in[4], (const char*)d_in[9], (char*)d_out);
}
```

```cpp
#include <hip/hip_runtime.h>
#include <hip/hip_bf16.h>
#include <stdint.h>
#include <utility>

typedef short    s16x4  __attribute__((ext_vector_type(4)));
typedef short    s16x8  __attribute__((ext_vector_type(8)));
typedef __bf16   bf16x2 __attribute__((ext_vector_type(2)));
typedef __bf16   bf16x8 __attribute__((ext_vector_type(8)));
typedef float    f32x2  __attribute__((ext_vector_type(2)));
typedef float    f32x4  __attribute__((ext_vector_type(4)));
typedef float    f32x16 __attribute__((ext_vector_type(16)));
typedef unsigned u32x2  __attribute__((ext_vector_type(2)));
typedef unsigned u32x4  __attribute__((ext_vector_type(4)));
typedef __attribute__((address_space(3))) s16x4* lds_s16x4_p;
typedef __attribute__((address_space(3))) char* lds_char_p;

#define DI __device__ __forceinline__
#define W_NT false
#define X_NT false
#define O_NT 0
#define ROT 1
#define QPW 2, 2, 0, 0, 0, 2, 2, 2, 1, 1, 0, 0, 0, 0, 0, 0
#define QPX 1, 1, 0, 0, 0, 1, 1, 1, 0, 1, 0, 0, 0, 0, 0, 0
#define QPE 0, 0, 4, 0, 0, 0, 0, 0, 0, 0, 4, 2, 2, 2, 1, 1
#define FIRST_NT false
#define ROTK 1
#define USE_PRIO 1
#define PRIO(n) __builtin_amdgcn_s_setprio(n)
#define WO_POL ""
#define OL_POL ""
#define SPREAD 1
#define TAIL 1
#define ACQ 0
#define ACQUIRE()
#define GRID1 256

constexpr int NB = 8, SQ = 144, DM = 512, NH = 32, SK = 512, DH = 128, HD = NH * DH;
constexpr int NQT = SQ / 16;
constexpr int TILE_B = 64 * 256;
constexpr int SLOT_B = 2 * TILE_B;
constexpr int LDS1 = 5 * SLOT_B;
constexpr int OH_B = NQT * 4096, OW_B = 2 * OH_B;
constexpr size_t PART_OFF = (size_t)256 * OW_B, FLAG_OFF = 2 * PART_OFF, FLAG2_OFF = FLAG_OFF + 256 * 128, GEN_OFF = FLAG2_OFF + 256 * 8 * 128;
constexpr int SPIN_MAX = 1 << 18;
constexpr float QSCALE = 0.08838834764831845f * 1.4426950408889634f;
constexpr float RESCALE_THR = 8.0f;

template <class F, int... Is> DI void static_for_impl(F&& f, std::integer_sequence<int, Is...>) { (f(std::integral_constant<int, Is>{}), ...); }
template <int N, class F> DI void static_for(F&& f) { static_for_impl(f, std::make_integer_sequence<int, N>{}); }
#define FOR_I(N) static_for<N>([&](auto ic_) __attribute__((always_inline)) { constexpr int i = decltype(ic_)::value;
#define END_FOR });

DI f32x4 mfma32(bf16x8 a, bf16x8 b, f32x4 c) { f32x4 d = __builtin_amdgcn_mfma_f32_16x16x32_bf16(a, b, c, 0, 0, 0); asm volatile("" :: "v"(d)); return d; }
DI f32x16 mfmaL(bf16x8 a, bf16x8 b, f32x16 c) { f32x16 d = __builtin_amdgcn_mfma_f32_32x32x16_bf16(a, b, c, 0, 0, 0); asm volatile("" :: "v"(d)); return d; }
DI f32x4 mfma16(s16x4 a, s16x4 b, f32x4 c) { f32x4 d = __builtin_amdgcn_mfma_f32_16x16x16bf16_1k(a, b, c, 0, 0, 0); asm volatile("" :: "v"(d)); return d; }
DI unsigned pk2(float lo, float hi) { f32x2 v = {lo, hi}; return __builtin_bit_cast(unsigned, __builtin_convertvector(v, bf16x2)); }
DI s16x4 tr_read(lds_char_p p) { return __builtin_amdgcn_ds_read_tr16_b64_v4i16((lds_s16x4_p)p); }
DI bf16x8 tr_frag(lds_char_p p, int second) {
  s16x4 lo = tr_read(p), hi = tr_read(p + second);
  return __builtin_bit_cast(bf16x8, __builtin_shufflevector(lo, hi, 0, 1, 2, 3, 4, 5, 6, 7));
}
DI bf16x8 pack8(f32x4 a, f32x4 b) {
  u32x4 w = {pk2(a[0], a[1]), pk2(a[2], a[3]), pk2(b[0], b[1]), pk2(b[2], b[3])};
  return __builtin_bit_cast(bf16x8, w);
}
DI float max3(float a, float b, float c) { return __builtin_fmaxf(__builtin_fmaxf(a, b), c); }
DI float xh_max(float x) {
  auto a = __builtin_amdgcn_permlane32_swap(__float_as_uint(x), __float_as_uint(x), false, false);
  return fmaxf(__uint_as_float(a[0]), __uint_as_float(a[1]));
}
DI float xh_sum(float x) {
  auto a = __builtin_amdgcn_permlane32_swap(__float_as_uint(x), __float_as_uint(x), false, false);
  return __uint_as_float(a[0]) + __uint_as_float(a[1]);
}
template <int B> DI bf16x8 pack8s(const f32x16& v, float k) {
  u32x4 w = {pk2(v[B] * k, v[B + 1] * k), pk2(v[B + 2] * k, v[B + 3] * k), pk2(v[B + 4] * k, v[B + 5] * k), pk2(v[B + 6] * k, v[B + 7] * k)};
  return __builtin_bit_cast(bf16x8, w);
}
DI float max16(float m, const f32x16& v) {
#pragma unroll
  for (int e = 0; e < 16; e += 2) m = max3(m, v[e], v[e + 1]);
  return m;
}
DI float xg_max(float x) {
  auto a = __builtin_amdgcn_permlane32_swap(__float_as_uint(x), __float_as_uint(x), false, false);
  x = fmaxf(__uint_as_float(a[0]), __uint_as_float(a[1]));
  auto b = __builtin_amdgcn_permlane16_swap(__float_as_uint(x), __float_as_uint(x), false, false);
  return fmaxf(__uint_as_float(b[0]), __uint_as_float(b[1]));
}
DI float xg_sum(float x) {
  auto a = __builtin_amdgcn_permlane32_swap(__float_as_uint(x), __float_as_uint(x), false, false);
  x = __uint_as_float(a[0]) + __uint_as_float(a[1]);
  auto b = __builtin_amdgcn_permlane16_swap(__float_as_uint(x), __float_as_uint(x), false, false);
  return __uint_as_float(b[0]) + __uint_as_float(b[1]);
}

template <int OFF, bool NT> DI void dma1(unsigned lds, const char* base, unsigned v) {
  lds = __builtin_amdgcn_readfirstlane(lds);
  if constexpr (NT) asm volatile("s_mov_b32 m0, %0\n\ts_nop 0\n\tglobal_load_lds_dwordx4 %2, %1 offset:%3 nt" :: "s"(lds), "s"(base), "v"(v), "i"(OFF) : "memory");
  else              asm volatile("s_mov_b32 m0, %0\n\ts_nop 0\n\tglobal_load_lds_dwordx4 %2, %1 offset:%3" :: "s"(lds), "s"(base), "v"(v), "i"(OFF) : "memory");
}
template <int OFF> DI void gload16nt(bf16x8& dst, const char* base, unsigned voff) {
  asm volatile("global_load_dwordx4 %0, %1, %2 offset:%3 nt" : "=v"(dst) : "v"(voff), "s"(base), "i"(OFF) : "memory");
}
DI void pin(bf16x8& x) { asm volatile("" : "+v"(x)); }
DI void keep(unsigned x) { asm volatile("" :: "v"(x)); }
DI void opaque(unsigned& x) { asm volatile("" : "+v"(x)); }
DI void dma_quarter(unsigned lds, const char* base, unsigned v) {
  lds = __builtin_amdgcn_readfirstlane(lds);
  asm volatile("s_mov_b32 m0, %0\n\ts_mov_b64 exec, 0xffff\n\tglobal_load_lds_dwordx4 %2, %1\n\ts_mov_b64 exec, -1" :: "s"(lds), "s"(base), "v"(v) : "memory");
}
DI void gload16_dev(bf16x8& dst, const char* base, unsigned voff) {
  asm volatile("global_load_dwordx4 %0, %1, %2 sc1" : "=v"(dst) : "v"(voff), "s"(base) : "memory");
}
DI void gload8_dev(u32x2& dst, const char* base, unsigned voff) {
  asm volatile("global_load_dwordx2 %0, %1, %2 sc1" : "=v"(dst) : "v"(voff), "s"(base) : "memory");
}
DI void dma_dev(unsigned lds, const char* base, unsigned v) {
  lds = __builtin_amdgcn_readfirstlane(lds);
  asm volatile("s_mov_b32 m0, %0\n\ts_nop 0\n\tglobal_load_lds_dwordx4 %2, %1 sc1" :: "s"(lds), "s"(base), "v"(v) : "memory");
}
DI void poll_tag(const char* base, unsigned voff, unsigned lo, unsigned hi) {
#pragma unroll 1
  for (int spin = 0; spin < SPIN_MAX; ++spin) {
    u32x2 f;
    asm volatile("global_load_dwordx2 %0, %1, %2 sc1\n\ts_waitcnt vmcnt(0)" : "=&v"(f) : "v"(voff), "s"(base) : "memory");
    if (__all(f[0] == lo && f[1] == hi)) break;
    __builtin_amdgcn_s_sleep(2);
  }
}
DI void gstore16_dev(char* p, bf16x8 v) { asm volatile("global_store_dwordx4 %0, %1, off sc1\n\ts_nop 1" :: "v"(p), "v"(v) : "memory"); }
DI void gstore8_dev(char* p, u32x2 v) { asm volatile("global_store_dwordx2 %0, %1, off sc1" :: "v"(p), "v"(v) : "memory"); }
#define WAIT_VM(n) asm volatile("s_waitcnt vmcnt(" #n ")" ::: "memory")
#define MEM_FENCE() asm volatile("" ::: "memory")
#define WAIT_LGKM(n) do { MEM_FENCE(); __builtin_amdgcn_s_waitcnt(0xC07F | ((n) << 8)); MEM_FENCE(); } while (0)
#define BARRIER() do { WAIT_LGKM(0); __builtin_amdgcn_s_barrier(); MEM_FENCE(); } while (0)
#define SCHED_FENCE() do { MEM_FENCE(); __builtin_amdgcn_sched_barrier(0); } while (0)

#define STAMP(i)
#define TICK(i)
#define STEPSTAMP(t)
#define DIAG_PARAMS
#define DIAG_ARGS
#define DIAG_DECL(n)
#define DIAG_FLUSH(n, ptr, slot)

DI bf16x8 q_frag(f32x4 t0, f32x4 t1) {
  unsigned a0 = pk2(t0[0] * QSCALE, t0[1] * QSCALE), a1 = pk2(t0[2] * QSCALE, t0[3] * QSCALE);
  unsigned b0 = pk2(t1[0] * QSCALE, t1[1] * QSCALE), b1 = pk2(t1[2] * QSCALE, t1[3] * QSCALE);
  auto p0 = __builtin_amdgcn_permlane32_swap(a0, b0, false, false);
  auto p1 = __builtin_amdgcn_permlane32_swap(a1, b1, false, false);
  auto r0 = __builtin_amdgcn_permlane16_swap(p0[0], p0[1], false, false);
  auto r1 = __builtin_amdgcn_permlane16_swap(p1[0], p1[1], false, false);
  u32x4 w = {r0[0], r1[0], r0[1], r1[1]};
  return __builtin_bit_cast(bf16x8, w);
}

DI lds_char_p lds_at(unsigned a) { return (lds_char_p)(uintptr_t)a; }
#define LDS_RD(T, p) (*(__attribute__((address_space(3))) T*)(p))
template <int N> DI void wait_vm() { asm volatile("s_waitcnt vmcnt(%0)" :: "i"(N) : "memory"); }

struct AttnState {
  bf16x8 qf[8], q9[4];
  f32x16 o[4];
  f32x4 o9[8];
  f32x16 c;
  float c9;
  float ls, ls9;
  f32x16 sE, sO;
  f32x4 s9;
  u32x4 pE[2];
  bf16x8 kc[4];
  unsigned rdk, rdk9, rdv, rdv9, vk0, ldsw;
  const char *kg, *vg;
  const char* wk;
  const char* wt;
  unsigned ldso;
  int hp;
  int rk;
};
#define EXP2X(v, e, dst, l) do { const float x0_ = __builtin_amdgcn_exp2f(v[e]); float x1_ = __builtin_amdgcn_exp2f(v[(e) + 1]); \
    dst = pk2(x0_, x1_); l += x0_; asm volatile("" : "+v"(l), "+v"(dst), "+v"(x1_)); v[(e) + 1] = x1_; } while (0)
#define ADD1(l, x) do { l += (x); asm volatile("" : "+v"(l)); } while (0)
#define MAX2X(m, v, e) do { m = max3(m, v[e], v[(e) + 1]); asm volatile("" : "+v"(m)); } while (0)
#define BF8(x) __builtin_bit_cast(bf16x8, x)
#define K_RD(kb, s) LDS_RD(bf16x8, (((s) & 1) ? ko : ke) + 8192 * (kb) + 256 * ((s) >> 1))
#define V_RD(kb, f) tr_frag(vr + 8192 * (kb) + 4096 * ((f) >> 2) + 256 * ((f) & 3), 2048)
template <int I> DI void dma_k(const AttnState& A, int slot, int t) {
  dma1<1024 * I, true>(A.ldsw + slot * SLOT_B, A.kg + ((t + A.rk) & 7) * TILE_B, A.vk0 ^ (16 * I));
}
template <int I> DI void dma_v(const AttnState& A, int slot, int t) {
  dma1<1024 * I, true>(A.ldsw + slot * SLOT_B, A.vg + ((t + A.rk) & 7) * TILE_B, A.vk0 ^ (32 * (I & 1)));
}
DI void dma_k4(const AttnState& A, int slot, int t) { dma_k<0>(A, slot, t); dma_k<1>(A, slot, t); dma_k<2>(A, slot, t); dma_k<3>(A, slot, t); }
DI void dma_v4(const AttnState& A, int slot, int t) { dma_v<0>(A, slot, t); dma_v<1>(A, slot, t); dma_v<2>(A, slot, t); dma_v<3>(A, slot, t); }
template <int I> DI void dma_kv(const AttnState& A, int slotk, int slotv, int t) {
  if constexpr (I < 4) dma_k<I>(A, slotk, t); else dma_v<I - 4>(A, slotv, t);
}
DI unsigned lane_id() {
  unsigned ln;
  asm volatile("v_mbcnt_lo_u32_b32 %0, -1, 0\n\tv_mbcnt_hi_u32_b32 %0, -1, %0" : "=v"(ln));
  return ln;
}
template <int I> DI void dma_wo(const AttnState& A, int peer, unsigned v) {
  dma1<0, false>(A.ldso + 1024 * I, A.wk + (size_t)(256 * peer + 16 * I) * DM * 2, v);
}
DI void rescale_own(AttnState& A, f32x16& s) {
  const float d = xh_max(max16(0.f, s)), a = __builtin_amdgcn_exp2f(-d);
  A.c -= d; s -= d; A.ls *= a;
#pragma unroll
  for (int i = 0; i < 4; ++i) A.o[i] *= a;
}
DI void rescale_shared(AttnState& A) {
  const float d = xg_max(max3(max3(0.f, A.s9[0], A.s9[1]), A.s9[2], A.s9[3])), a = __builtin_amdgcn_exp2f(-d);
  A.c9 -= d; A.s9 -= d; A.ls9 *= a;
#pragma unroll
  for (int i = 0; i < 8; ++i) A.o9[i] *= a;
}
DI void attn_x2a(AttnState& A, lds_char_p ke, lds_char_p ko, bf16x8* k2) {
  FOR_I(4)
    A.sO = mfmaL(k2[i], A.qf[i], i ? A.sO : A.c);
    A.kc[i] = K_RD(1, 4 + i);
    EXP2X(A.sE, 2 * i, A.pE[0][i], A.ls);
    SCHED_FENCE();
  END_FOR
}
template <int MODE> DI void attn_step(AttnState& A, int t, int sk DIAG_PARAMS) {
  constexpr bool LAST = MODE == 2;
  const int sv = sk == 4 ? 0 : sk + 1, sn = sv == 4 ? 0 : sv + 1, sp = sk == 0 ? 4 : sk - 1;
  TICK(0);
  wait_vm<(LAST ? 1 : MODE == 1 ? 0 : 4)>();
  TICK(1);
  BARRIER();
  TICK(2);
  PRIO(3);
  lds_char_p vr = lds_at(A.rdv + sv * SLOT_B);
  bf16x8 va[8], vb[8], k0[8], k2[4], k9[4];
  s16x4 v9[8];
  u32x4 pO[2];
  u32x2 p9;
  float mx;
  bool grow;
  FOR_I(4)
    A.sO = mfmaL(A.kc[i], A.qf[4 + i], A.sO);
    if constexpr (i < 2) { va[2 * i] = V_RD(0, 2 * i); va[2 * i + 1] = V_RD(0, 2 * i + 1); }
    else va[2 + i] = V_RD(0, 2 + i);
    EXP2X(A.sE, 8 + 2 * i, A.pE[1][i], A.ls);
    SCHED_FENCE();
  END_FOR
  lds_char_p ve9 = lds_at(A.rdv9 + sv * SLOT_B), vo9 = lds_at((A.rdv9 + sv * SLOT_B) ^ 32);
  lds_char_p ke = lds_at(A.rdk + sn * SLOT_B), ko = lds_at((A.rdk + sn * SLOT_B) ^ 32);
  FOR_I(8)
    A.o[i & 3] = mfmaL(va[i], BF8(A.pE[i >> 2]), A.o[i & 3]);
    if constexpr (i < 2) va[6 + i] = V_RD(0, 6 + i);
    else if constexpr (i < 6) { v9[2 * (i - 2)] = tr_read(ve9 + 256 * (i - 2)); v9[2 * (i - 2) + 1] = tr_read(vo9 + 256 * (i - 2)); }
    else if constexpr (!LAST) k0[i - 6] = K_RD(0, i - 6);
    if constexpr (MODE == 0 && !SPREAD) dma_kv<i>(A, sp, sk, t + 2);
    if constexpr ((MODE == 0 || MODE == 3) && SPREAD && (i & 1) == 0) dma_k<(i >> 1)>(A, sp, t + 2);
    if constexpr (LAST) dma_wo<i>(A, A.hp, A.vk0);
    if constexpr (TAIL && MODE == 1 && (i & 1) == 0) dma_v<(i >> 1)>(A, sk < 2 ? sk + 3 : sk - 2, t + 1);
    if constexpr (MODE == 1 && i == 7) {
      const unsigned l = __builtin_amdgcn_readfirstlane(A.ldso - 2 * SLOT_B);
      asm volatile("s_mov_b32 m0, %0\n\ts_nop 0\n\tglobal_load_lds_dword %2, %1" :: "s"(l), "s"(A.wt), "v"(lane_id() * 64) : "memory");
    }
    ADD1(A.ls, A.sE[2 * i + 1]);
    if constexpr (i == 7) PRIO(2);
    if constexpr (i == 0) { mx = 0.f; EXP2X(A.s9, 0, p9[0], A.ls9); }
    if constexpr (i == 1) EXP2X(A.s9, 2, p9[1], A.ls9);
    if constexpr (i >= 2) MAX2X(mx, A.sO, 2 * (i - 2));
    SCHED_FENCE();
  END_FOR
  FOR_I(4)
    A.o9[2 * i] = mfma16(v9[2 * i], __builtin_bit_cast(s16x4, p9), A.o9[2 * i]);
    if constexpr (LAST) { vb[2 * i] = V_RD(1, 2 * i); vb[2 * i + 1] = V_RD(1, 2 * i + 1); }
    else k0[2 + i] = K_RD(0, 2 + i);
    A.o9[2 * i + 1] = mfma16(v9[2 * i + 1], __builtin_bit_cast(s16x4, p9), A.o9[2 * i + 1]);
    if constexpr (i < 2) { MAX2X(mx, A.sO, 12 + 2 * i); ADD1(A.ls9, A.s9[2 * i + 1]); }
    if constexpr (i == 2) grow = __any(mx > RESCALE_THR);
    SCHED_FENCE();
  END_FOR
  if (__builtin_expect(grow, 0)) rescale_own(A, A.sO);
  SCHED_FENCE();
  lds_char_p kn9 = lds_at(A.rdk9 + sn * SLOT_B);
  FOR_I(8)
    if constexpr (!LAST) {
      A.sE = mfmaL(k0[i], A.qf[i], i ? A.sE : A.c);
      if constexpr (i < 2) k0[6 + i] = K_RD(0, 6 + i);
      else if constexpr (i < 6) k9[i - 2] = LDS_RD(bf16x8, kn9 + 256 * (i - 2));
      else vb[i - 6] = V_RD(1, i - 6);
    }
    EXP2X(A.sO, 2 * i, pO[i >> 2][i & 3], A.ls);
    if constexpr (i == 6) PRIO(1);
    if constexpr (MODE == 0 && SPREAD && (i == 0 || i == 5)) dma_v<(i ? 1 : 0)>(A, sk, t + 2);
    SCHED_FENCE();
  END_FOR
  if constexpr (!LAST) {
    FOR_I(2)
      A.s9 = mfma32(k9[2 * i], A.q9[2 * i], i ? A.s9 : f32x4{A.c9, A.c9, A.c9, A.c9});
      vb[2 + 2 * i] = V_RD(1, 2 + 2 * i);
      A.s9 = mfma32(k9[2 * i + 1], A.q9[2 * i + 1], A.s9);
      vb[3 + 2 * i] = V_RD(1, 3 + 2 * i);
      SCHED_FENCE();
    END_FOR
  }
  FOR_I(8)
    A.o[i & 3] = mfmaL(vb[i], BF8(pO[i >> 2]), A.o[i & 3]);
    if constexpr (!LAST) {
      if constexpr (i < 2) vb[6 + i] = V_RD(1, 6 + i);
      else if constexpr (i < 6) k2[i - 2] = K_RD(1, i - 2);
    }
    ADD1(A.ls, A.sO[2 * i + 1]);
    if constexpr (i == 6) PRIO(0);
    if constexpr (MODE == 0 && SPREAD && (i == 0 || i == 5)) dma_v<(i ? 3 : 2)>(A, sk, t + 2);
    if constexpr (!LAST) {
      if constexpr (i == 0) mx = 0.f;
      if constexpr (i < 4) { MAX2X(mx, A.sE, 4 * i); MAX2X(mx, A.sE, 4 * i + 2); }
      if constexpr (i == 4 || i == 5) MAX2X(mx, A.s9, 2 * (i - 4));
      if constexpr (i == 6) grow = __any(mx > RESCALE_THR);
    }
    SCHED_FENCE();
  END_FOR
  if constexpr (!LAST) {
    if (__builtin_expect(grow, 0)) { rescale_own(A, A.sE); rescale_shared(A); }
    SCHED_FENCE();
    attn_x2a(A, ke, ko, k2);
  }
}

constexpr int QP_W[16] = {QPW}, QP_X[16] = {QPX}, QP_E[16] = {QPE};
constexpr int qp_sum(const int* t, int s) { int n = 0; for (int j = 0; j < s; ++j) n += t[j]; return n; }
constexpr int qp_w0(int s) { return 4 + qp_sum(QP_W, s); }
constexpr int qp_x0(int s) { return 2 + qp_sum(QP_X, s); }
constexpr int qp_e0(int s) { return qp_sum(QP_E, s); }
constexpr int qp_n(int s) { return 2 * QP_W[s] + 3 * QP_X[s] + QP_E[s]; }
constexpr int qp_cum(int s) { int n = 14; for (int j = 0; j < s; ++j) n += qp_n(j); return n; }
constexpr int qp_endw(int i) {
  if (i <= 3) return 7 * (i >> 1) + 5 + 2 * (i & 1);
  int s = 0; while (qp_w0(s + 1) <= i) ++s;
  return qp_cum(s) + 2 * (i - qp_w0(s) + 1);
}
constexpr int qp_endx(int c) {
  if (c <= 1) return 7 * c + 3;
  int s = 0; while (qp_x0(s + 1) <= c) ++s;
  return qp_cum(s) + 2 * QP_W[s] + 3 * (c - qp_x0(s) + 1);
}
constexpr int qp_ende(int e) { int s = 0; while (qp_e0(s + 1) <= e) ++s; return qp_cum(s) + 2 * QP_W[s] + 3 * QP_X[s] + e - qp_e0(s) + 1; }
constexpr int qp_max(int a, int b) { return a > b ? a : b; }
constexpr int qp_wait(int s) {
  const int need = s <= 14 ? qp_max(qp_endw(s + 1), qp_endx((s + 1) >> 1)) : 0;
  return qp_cum(s) - qp_max(need, s == 14 ? qp_ende(3) : s == 15 ? qp_ende(7) : 0);
}
constexpr bool qp_ok() {
  if (qp_w0(16) != 16 || qp_x0(16) != 8 || qp_e0(16) != 16 || qp_e0(13) > 12) return false;
  for (int s = 0; s < 16; ++s) {
    if (qp_wait(s) < 0 || qp_wait(s) > 63) return false;
    if (qp_w0(s + 1) - 1 > s + 6) return false;
    if (qp_x0(s + 1) - 1 >= 3 && 2 * (qp_x0(s + 1) - 4) + 1 > s) return false;
  }
  return true;
}
static_assert(qp_ok(), "projection schedule");

__global__ __launch_bounds__(512, 2)
void fused_kernel(const char* __restrict__ xA, const char* __restrict__ kA, const char* __restrict__ vA, const char* __restrict__ wqA,
                  const char* __restrict__ woA, const char* __restrict__ xB, const char* __restrict__ kB, const char* __restrict__ vB,
                  const char* __restrict__ wqB, const char* __restrict__ woB, char* ows, char* __restrict__ out) {
  extern __shared__ __attribute__((aligned(1024))) char smem_raw[];
  const int tid = threadIdx.x, lane = tid & 63, r = lane & 15, g = lane >> 4, r32 = lane & 31, hh = lane >> 5;
  const int wv = __builtin_amdgcn_readfirstlane(tid >> 6), w = wv & 3, gq = wv >> 2;
  const unsigned ldsb = (unsigned)(uintptr_t)(lds_char_p)smem_raw, lds0 = ldsb + gq * TILE_B;
  const int bid = blockIdx.x, xcd = bid & 7, idx = bid >> 3;
  const int br = xcd >> 2, h = 8 * (xcd & 3) + 2 * (idx & 3) + gq, b = idx >> 2;
  const int rot = ROT ? (b + 2 * (idx & 3)) & 7 : 0;
  const char* xg = (br ? xB : xA) + (size_t)b * SQ * DM * 2;
  const char* wg = (br ? wqB : wqA) + h * DH * 2;
  const int q4 = (lane >> 2) & 3, p4 = lane & 3, r8 = (lane >> 2) & 7, blk = g & 1;

  AttnState A;
  A.kg = (br ? kB : kA) + (size_t)(b * NH + h) * SK * DH * 2;
  A.vg = (br ? vB : vA) + (size_t)(b * NH + h) * SK * DH * 2;
  A.rk = ROTK ? (h + b) & 7 : 0;
  A.ldsw = lds0 + w * 4096;
  A.vk0 = (16 * w + q4) * 256 + 64 * g + 16 * p4;
  DIAG_DECL(10);
  STAMP(0);
  unsigned tag_lo, tag_hi;
  u32x2 gen;
  gload8_dev(gen, ows + GEN_OFF, 0);
  {
    constexpr int XOFF = 3 * SLOT_B, XCH = SQ * 128, EXOFF = XOFF + 3 * XCH;
    const unsigned vw0 = (8 * w + r8) * (HD * 2) + 64 * hh + 16 * (p4 ^ (2 * (w & 1)));
#define WPIECE(s) (((((s) + 2) % 6) >> 1) * SLOT_B + ((s) & 1) * 8192)
#define DMA_W(s, i) dma1<128 * (i), (FIRST_NT && (s) < 2)>(lds0 + WPIECE(s) + w * 2048 + 896 * (i), wg + (size_t)(((s) + 2 * rot) & 15) * 32 * HD * 2, vw0)
    unsigned vx[3];
    FOR_I(3) const int row = 18 * wv + 8 * i + (i < 2 ? lane >> 3 : (lane >> 3) & 1);
             vx[i] = row * (DM * 2) + 16 * ((lane & 7) ^ ((row >> 1) & 7)); END_FOR
#define XPOS(c) ((((c) + 1) % 3) * XCH)
#define DMA_X(c, i) do { const unsigned l_ = ldsb + XOFF + XPOS(c) + wv * (18 * 128) + 1024 * (i); const char* s_ = xg + 128 * (((c) + rot) & 7); \
      if constexpr ((i) < 2) dma1<0, (FIRST_NT && (c) < 2)>(l_, s_, vx[i]); else dma_quarter(l_, s_, vx[2]); } while (0)
    unsigned xr[6];
    FOR_I(4) xr[i] = (ldsb + XOFF + (32 * w + r32) * 128 + 16 * (hh ^ ((r32 >> 1) & 7))) ^ (32 * i); END_FOR
    FOR_I(2) xr[4 + i] = (ldsb + XOFF + (128 + r) * 128 + 16 * (g ^ (r >> 1))) ^ (64 * i); END_FOR
#define X_RD(set, s, i) xf[set][i] = LDS_RD(bf16x8, lds_at(xr[(i) < 2 ? 2 * ((s) & 1) + (i) : 4 + ((s) & 1)]) + XPOS((s) >> 1))
    unsigned rw[6];
    rw[0] = lds0 + 2048 * hh + 64 * q4 + 32 * (blk ^ hh) + 8 * (((p4 & 1) << 1) | (p4 >> 1));
    rw[1] = lds0 + 2048 * g + 64 * q4 + 32 * blk + 8 * p4 + 512 * w; rw[2] = rw[1] ^ 32;
    FOR_I(3) rw[3 + i] = rw[i] + 2 * SLOT_B; END_FOR
    FOR_I(6) opaque(rw[i]); opaque(xr[i]); END_FOR
#define W_RD(set, s, i) wf[set][i] = tr_frag(lds_at(rw[((i) < 8 ? 0 : 1 + ((i) & 1)) + (WPIECE(s) >= 2 * SLOT_B ? 3 : 0)]) + \
                                             (((i) < 8 ? 4096 * (((i) >> 2) & 1) + 512 * ((i) & 3) : 0) + WPIECE(s) % (2 * SLOT_B)), 256)
    bf16x8 er[12];
    auto early = [&](auto ec_) __attribute__((always_inline)) {
      constexpr int e = decltype(ec_)::value, j = e & 3;
      if constexpr (e >= 4 && e < 8) gload16nt<1024 * j>(er[e], A.vg + A.rk * TILE_B, A.vk0 ^ (32 * (j & 1)));
      else if constexpr (e < 12) gload16nt<1024 * j>(er[e], A.kg + (((e >> 3) + A.rk) & 7) * TILE_B, A.vk0 ^ (16 * j));
      else dma_v<j>(A, 3, 1);
    };
    lds_char_p dump = lds_at(A.ldsw + lane * 16);
    bf16x8 xf[2][3], wf[2][10];
    f32x16 acc[4];
    f32x4 acc9[2];
#pragma unroll
    for (int i = 0; i < 4; ++i) acc[i] = f32x16{};
    acc9[0] = f32x4{0, 0, 0, 0}; acc9[1] = f32x4{0, 0, 0, 0};

    FOR_I(2)
      DMA_X(i, 0); DMA_X(i, 1); DMA_X(i, 2);
      DMA_W(2 * i, 0); DMA_W(2 * i, 1); DMA_W(2 * i + 1, 0); DMA_W(2 * i + 1, 1);
    END_FOR
    wait_vm<9>();
    asm volatile("" : "+v"(gen));
    tag_lo = __builtin_amdgcn_readfirstlane(gen[0]) + 1u;
    tag_hi = __builtin_amdgcn_readfirstlane(gen[1]);
    asm volatile("" : "+s"(tag_lo), "+s"(tag_hi));
    BARRIER();
    STAMP(1);
    FOR_I(3) X_RD(0, 0, i); END_FOR
    FOR_I(10) W_RD(0, 0, i); END_FOR
    static_for<16>([&](auto sc_) __attribute__((always_inline)) {
      constexpr int s = decltype(sc_)::value;
      constexpr int cs = s & 1, ns = cs ^ 1;
      wait_vm<qp_wait(s)>();
      BARRIER();
      if constexpr (s == 8) STAMP(2);
      SCHED_FENCE();
      FOR_I(9)
        if constexpr (i == 0) PRIO(3);
        if constexpr (i == 3) PRIO(2);
        if constexpr (i == 5) PRIO(1);
        if constexpr (i == 7) PRIO(0);
        if constexpr (i < 8) acc[i & 3] = mfmaL(wf[cs][i], xf[cs][i >> 2], acc[i & 3]);
        else { acc9[0] = mfma32(wf[cs][8], xf[cs][2], acc9[0]); acc9[1] = mfma32(wf[cs][9], xf[cs][2], acc9[1]); }
        SCHED_FENCE();
        if constexpr (s < 15) {
          if constexpr (i < 7) W_RD(ns, s + 1, i);
          if constexpr (i < 6) { if constexpr (i & 1) W_RD(ns, s + 1, 7 + (i >> 1)); else X_RD(ns, s + 1, i >> 1); }
        }
        static_for<qp_n(s)>([&](auto kc_) __attribute__((always_inline)) {
          constexpr int k = decltype(kc_)::value, kx = k - 2 * QP_W[s], ke = kx - 3 * QP_X[s];
          if constexpr (k * 9 / qp_n(s) == i) {
            if constexpr (kx < 0) DMA_W(qp_w0(s) + (k >> 1), k & 1);
            else if constexpr (ke < 0) DMA_X(qp_x0(s) + kx / 3, kx % 3);
            else early(std::integral_constant<int, qp_e0(s) + ke>{});
          }
        });
        if constexpr (s >= 14 && i < 4) { pin(er[4 * (s - 14) + i]); LDS_RD(bf16x8, dump + (s - 14) * SLOT_B + 1024 * i) = er[4 * (s - 14) + i]; }
        SCHED_FENCE();
      END_FOR
    });
    lds_char_p ex = lds_at(ldsb + EXOFF + gq * 4096 + lane * 16);
    LDS_RD(bf16x8, ex + w * 1024) = q_frag(acc9[0], acc9[1]);
    FOR_I(8) A.qf[i] = pack8s<8 * (i & 1)>(acc[i >> 1], QSCALE); END_FOR
    wait_vm<qp_cum(16) - qp_ende(11)>();
    FOR_I(4) pin(er[8 + i]); LDS_RD(bf16x8, dump + 2 * SLOT_B + 1024 * i) = er[8 + i]; END_FOR
    BARRIER();
    FOR_I(4) A.q9[i] = LDS_RD(bf16x8, ex + i * 1024); END_FOR
#undef WPIECE
#undef DMA_W
#undef XPOS
#undef DMA_X
#undef X_RD
#undef W_RD
  }

  STAMP(3);
#pragma unroll
  for (int i = 0; i < 4; ++i) A.o[i] = f32x16{};
#pragma unroll
  for (int i = 0; i < 8; ++i) A.o9[i] = f32x4{0, 0, 0, 0};
  A.rdk = lds0 + 1024 * (r32 >> 2) + 64 * (r32 & 3) + 16 * (hh ^ ((r32 >> 2) & 3));
  A.rdk9 = lds0 + 4096 * w + 1024 * (r >> 2) + 64 * (r & 3) + 16 * (g ^ (r >> 2));
  A.rdv = lds0 + 1024 * hh + 64 * q4 + 32 * (blk ^ hh) + 8 * p4;
  A.rdv9 = lds0 + 4096 * w + 1024 * g + 64 * q4 + 32 * blk + 8 * p4;
  {
    lds_char_p ke = lds_at(A.rdk), ko = lds_at(A.rdk ^ 32);
    bf16x8 k0[8], k2[4], k9[4];
    A.ls = 0.f; A.ls9 = 0.f;
    FOR_I(8) k0[i] = K_RD(0, i); END_FOR
    FOR_I(4) k9[i] = LDS_RD(bf16x8, lds_at(A.rdk9) + 256 * i); k2[i] = K_RD(1, i); END_FOR
    A.sE = f32x16{}; A.s9 = f32x4{0, 0, 0, 0};
    FOR_I(8) A.sE = mfmaL(k0[i], A.qf[i], A.sE); END_FOR
    FOR_I(4) A.s9 = mfma32(k9[i], A.q9[i], A.s9); END_FOR
    const float m = xh_max(max16(A.sE[0], A.sE));
    A.c = f32x16{} - m; A.sE -= m;
    const float m9 = xg_max(fmaxf(max3(A.s9[0], A.s9[1], A.s9[2]), A.s9[3]));
    A.c9 = -m9; A.s9 -= m9;
    SCHED_FENCE();
    attn_x2a(A, ke, ko, k2);
  }
  TICK(7);
  int sk = 0;
#pragma unroll 1
  for (int t = 0; t < (TAIL ? 5 : 6); ++t) {
    asm volatile("" : "+v"(A.vk0), "+s"(sk));
    attn_step<0>(A, t, sk DIAG_ARGS);
    sk = sk >= 3 ? sk - 3 : sk + 2;
  }
  const int hg = xcd & 3;
  A.hp = idx & 3;
  A.wt = (br ? woB : woA) + (size_t)hg * 1024 * DM * 2 + (idx * 8 + wv) * 4096;
  A.wk = (br ? woB : woA) + (size_t)(1024 * hg + 128 * gq) * DM * 2 + A.hp * 256 + w * 64;
  A.ldso = ldsb + 3 * SLOT_B + wv * 8192;
  attn_step<3>(A, 5, sk DIAG_ARGS); sk += 2;
  attn_step<1>(A, 6, sk DIAG_ARGS);
  { const unsigned l = lane_id(); A.vk0 = (l >> 2) * (DM * 2) + 16 * ((l & 3) ^ ((l >> 3) & 2)); }
  attn_step<2>(A, 7, sk + 2 DIAG_ARGS);
  TICK(0);

  STAMP(4);
  const int ln = lane_id(), lr = ln & 15, lg = ln >> 4;
  char* ob = ows + (size_t)bid * OW_B + gq * OH_B;
  lds_char_p xo = lds_at(ldsb + SLOT_B + gq * SLOT_B);
  const unsigned srcoff = (2 * w + (lg >> 1)) * 1024 + (16 * (lg & 1) + lr) * 16;
  const unsigned ooff = (2 * w + (lg & 1)) * 4096 + (lg >> 1) * 256 + lr * 16, ooff9 = 8 * 4096 + w * 1024 + ln * 16;
  const unsigned vwo = A.vk0;
  const int hp = A.hp;
  bf16x8 of[9], wa[4][8];
  lds_char_p wrd = lds_at(A.ldso + 64 * (16 * (lg >> 1) + 4 * (lg & 1) + ((ln >> 2) & 3)) + 32 * (lg & 1) + 8 * (ln & 3));
  lds_char_p wrd1 = lds_at((unsigned)(uintptr_t)wrd ^ 32);
#define WO_TAKE(j) do { FOR_I(8) wa[j][i] = tr_frag(((i & 1) ? wrd1 : wrd) + 2048 * (i >> 1), 512); END_FOR WAIT_LGKM(0); } while (0)
#define WO_NEXT(j) FOR_I(8) dma_wo<i>(A, (hp + (j)) & 3, vwo); END_FOR
  {
    f32x2 st[4];
    f32x4 u0[4], u1[4];
    st[0] = f32x2{A.c9, xg_sum(A.ls9)};
    wait_vm<8>();
    FOR_I(8) LDS_RD(f32x4, xo + (w * 8 + i) * 1024 + ln * 16) = A.o9[i]; END_FOR
    MEM_FENCE();
    u0[0] = LDS_RD(f32x4, xo + w * 8192 + srcoff); u1[0] = LDS_RD(f32x4, xo + w * 8192 + srcoff + 512);
    const float inv = 1.0f / xh_sum(A.ls);
    FOR_I(8) of[i] = pack8s<8 * (i & 1)>(A.o[i >> 1], inv); gstore16_dev(ob + ooff + 512 * i, of[i]); END_FOR
    WAIT_LGKM(0);
    LDS_RD(f32x2, xo + w * 10240 + ln * 8) = st[0];
    BARRIER();
    FOR_I(3)
      const int j = (w + 1 + i) & 3;
      st[1 + i] = LDS_RD(f32x2, xo + j * 10240 + ln * 8);
      u0[1 + i] = LDS_RD(f32x4, xo + j * 8192 + srcoff); u1[1 + i] = LDS_RD(f32x4, xo + j * 8192 + srcoff + 512);
    END_FOR
    const float nmin = fminf(fminf(st[0][0], st[1][0]), fminf(st[2][0], st[3][0]));
    float f[4], l = 0.f;
    FOR_I(4) f[i] = __builtin_amdgcn_exp2f(nmin - st[i][0]); l += f[i] * st[i][1]; END_FOR
    const float inv9 = 1.0f / l;
    f32x4 t0 = {0, 0, 0, 0}, t1 = {0, 0, 0, 0};
    FOR_I(4) t0 += u0[i] * (f[i] * inv9); t1 += u1[i] * (f[i] * inv9); END_FOR
    of[8] = pack8(t0, t1);
    gstore16_dev(ob + ooff9, of[8]);
  }
  wait_vm<9>();
  WO_TAKE(0);
  WO_NEXT(1)
  BARRIER();
  FOR_I(8) LDS_RD(bf16x8, lds_at(ldsb + gq * OH_B + ooff) + 512 * i) = of[i]; END_FOR
  LDS_RD(bf16x8, lds_at(ldsb + gq * OH_B + ooff9)) = of[8];
  STAMP(5);
  wait_vm<0>();
  BARRIER();
  if (wv == 0 && ln == 0) gstore8_dev(ows + FLAG_OFF + bid * 128, u32x2{tag_lo, tag_hi});
  STAMP(6);
  f32x4 acc[9][2];
  FOR_I(18) acc[i >> 1][i & 1] = f32x4{0, 0, 0, 0}; END_FOR
  auto oproj = [&](auto jc_, auto s0c_, auto nsc_, unsigned region) __attribute__((always_inline)) {
    constexpr int j = decltype(jc_)::value, S0 = decltype(s0c_)::value, NF = 9 * decltype(nsc_)::value, AH = 5;
    lds_char_p src = lds_at(ldsb + region + gq * OH_B + ln * 16);
    bf16x8 bq[NF];
    FOR_I(AH) bq[i] = LDS_RD(bf16x8, src + (i % 9) * 4096 + (S0 + i / 9) * 1024); END_FOR
    SCHED_FENCE();
    FOR_I(NF)
      acc[i % 9][0] = mfma32(wa[j][2 * (S0 + i / 9)], bq[i], acc[i % 9][0]);
      if constexpr (i + AH < NF) bq[i + AH] = LDS_RD(bf16x8, src + ((i + AH) % 9) * 4096 + (S0 + (i + AH) / 9) * 1024);
      acc[i % 9][1] = mfma32(wa[j][2 * (S0 + i / 9) + 1], bq[i], acc[i % 9][1]);
      SCHED_FENCE();
    END_FOR
  };
#define IC(n) std::integral_constant<int, n>{}
#define O_FETCH(j, region) FOR_I(9) dma_dev(ldsb + (region) + wv * 9216 + 1024 * i, ows + (size_t)(bid + 8 * (((hp + (j)) & 3) - hp)) * OW_B + wv * 9216 + 1024 * i, ln * 16); END_FOR
  WO_TAKE(1);
  WO_NEXT(2)
  oproj(IC(0), IC(0), IC(2), 0);
  wait_vm<0>();
  WO_TAKE(2);
  WO_NEXT(3)
  oproj(IC(0), IC(2), IC(2), 0);
  wait_vm<0>();
  WO_TAKE(3);
  const char* flags = ows + FLAG_OFF;
  if (bid == 0 && wv == 7) {
#pragma unroll 1
    for (int spin = 0; spin < SPIN_MAX; ++spin) {
      u32x2 f0, f1, f2, f3;
      asm volatile("global_load_dwordx2 %0, %4, %5 sc1\n\tglobal_load_dwordx2 %1, %4, %5 offset:128 sc1\n\t"
                   "global_load_dwordx2 %2, %4, %5 offset:256 sc1\n\tglobal_load_dwordx2 %3, %4, %5 offset:384 sc1\n\ts_waitcnt vmcnt(0)"
                   : "=&v"(f0), "=&v"(f1), "=&v"(f2), "=&v"(f3) : "v"(ln * 512), "s"(flags) : "memory");
      const unsigned dl = (f0[0] ^ tag_lo) | (f1[0] ^ tag_lo) | (f2[0] ^ tag_lo) | (f3[0] ^ tag_lo);
      const unsigned dh = (f0[1] ^ tag_hi) | (f1[1] ^ tag_hi) | (f2[1] ^ tag_hi) | (f3[1] ^ tag_hi);
      if (__all((dl | dh) == 0)) break;
      __builtin_amdgcn_s_sleep(2);
    }
    if (ln == 0) gstore8_dev(ows + GEN_OFF, u32x2{tag_lo, tag_hi});
  } else {
    poll_tag(flags, (bid + 8 * ((ln & 3) - hp)) * 128, tag_lo, tag_hi);
  }
  ACQUIRE();
  STAMP(7);
  BARRIER();
  O_FETCH(1, OW_B)
  O_FETCH(2, 0)
  wait_vm<9>();
  BARRIER();
  oproj(IC(1), IC(0), IC(4), OW_B);
  wait_vm<0>();
  BARRIER();
  O_FETCH(3, OW_B)
  oproj(IC(2), IC(0), IC(4), 0);
  wait_vm<0>();
  BARRIER();
  oproj(IC(3), IC(0), IC(4), OW_B);
  STAMP(8);
  f32x4 res[9];
  FOR_I(9) LDS_RD(f32x4, lds_at(ldsb + wv * 9216 + ln * 16) + 1024 * i) = gq ? acc[i][0] : acc[i][1]; res[i] = gq ? acc[i][1] : acc[i][0]; END_FOR
  BARRIER();
  FOR_I(9) res[i] += LDS_RD(f32x4, lds_at(ldsb + (wv ^ 4) * 9216 + ln * 16) + 1024 * i); END_FOR
  if (w != hg) {
    char* dst = ows + PART_OFF + (size_t)bid * OW_B + wv * 9216 + ln * 16;
    FOR_I(9) gstore16_dev(dst + 1024 * i, BF8(res[i])); END_FOR
    wait_vm<0>();
    if (ln == 0) gstore8_dev(ows + FLAG2_OFF + (bid * 8 + wv) * 128, u32x2{tag_lo, tag_hi});
  } else {
    const int o3 = (hg + 1 + (ln & 3) % 3) & 3;
    poll_tag(ows + FLAG2_OFF, ((bid - hg + o3) * 8 + wv) * 128, tag_lo, tag_hi);
    ACQUIRE();
    bf16x8 in[27];
    FOR_I(27) gload16_dev(in[i], ows + PART_OFF + (size_t)(bid - hg + ((hg + 1 + i / 9) & 3)) * OW_B + wv * 9216 + 1024 * (i % 9), ln * 16); END_FOR
    wait_vm<0>();
    FOR_I(27) pin(in[i]); res[i % 9] += __builtin_bit_cast(f32x4, in[i]); END_FOR
    char* outp = out + ((size_t)(b * SQ + lr) * (2 * DM) + br * DM + 128 * hp + 32 * w + 16 * gq + 4 * lg) * 2;
    FOR_I(9) *(u32x2*)(outp + (size_t)(16 * i) * (2 * DM * 2)) = u32x2{pk2(res[i][0], res[i][1]), pk2(res[i][2], res[i][3])}; END_FOR
  }
#undef WO_TAKE
#undef WO_NEXT
#undef O_FETCH
#undef IC
  STAMP(9);
}

extern "C" void kernel_launch(void* const* d_in, const int* in_sizes, int n_in,
                              void* d_out, int out_size, void* d_ws, size_t ws_size,
                              hipStream_t stream) {
  (void)in_sizes; (void)n_in; (void)out_size; (void)ws_size;
  (void)hipFuncSetAttribute((const void*)fused_kernel, hipFuncAttributeMaxDynamicSharedMemorySize, LDS1);
  hipLaunchKernelGGL(fused_kernel, dim3(256), dim3(512), LDS1, stream,
                     (const char*)d_in[0], (const char*)d_in[1], (const char*)d_in[2], (const char*)d_in[3], (const char*)d_in[4],
                     (const char*)d_in[5], (const char*)d_in[6], (const char*)d_in[7], (const char*)d_in[8], (const char*)d_in[9],
                     (char*)d_ws, (char*)d_out);
}
```

```cpp
#include <hip/hip_runtime.h>
#include <hip/hip_bf16.h>
#include <stdint.h>
#include <utility>

typedef short    s16x4  __attribute__((ext_vector_type(4)));
typedef short    s16x8  __attribute__((ext_vector_type(8)));
typedef __bf16   bf16x2 __attribute__((ext_vector_type(2)));
typedef __bf16   bf16x8 __attribute__((ext_vector_type(8)));
typedef float    f32x2  __attribute__((ext_vector_type(2)));
typedef float    f32x4  __attribute__((ext_vector_type(4)));
typedef float    f32x16 __attribute__((ext_vector_type(16)));
typedef unsigned u32x2  __attribute__((ext_vector_type(2)));
typedef unsigned u32x4  __attribute__((ext_vector_type(4)));
typedef __attribute__((address_space(3))) s16x4* lds_s16x4_p;
typedef __attribute__((address_space(3))) char* lds_char_p;

#define DI __device__ __forceinline__
#define W_NT false
#define X_NT false
#define O_NT 0
#define ROT 1
#define KV0 12
#define ROTK 1
#define USE_PRIO 1
#define PRIO(n) __builtin_amdgcn_s_setprio(n)
#define GRID1 256

constexpr int NB = 8, SQ = 144, DM = 512, NH = 32, SK = 512, DH = 128, HD = NH * DH;
constexpr int NQT = SQ / 16;
constexpr int TILE_B = 64 * 256;
constexpr int SLOT_B = 2 * TILE_B;
constexpr int LDS1 = 5 * SLOT_B;
constexpr int LDS2 = 8 * 20480;
constexpr float QSCALE = 0.08838834764831845f * 1.4426950408889634f;
constexpr float RESCALE_THR = 8.0f;

template <class F, int... Is> DI void static_for_impl(F&& f, std::integer_sequence<int, Is...>) { (f(std::integral_constant<int, Is>{}), ...); }
template <int N, class F> DI void static_for(F&& f) { static_for_impl(f, std::make_integer_sequence<int, N>{}); }
#define FOR_I(N) static_for<N>([&](auto ic_) __attribute__((always_inline)) { constexpr int i = decltype(ic_)::value;
#define END_FOR });

DI f32x4 mfma32(bf16x8 a, bf16x8 b, f32x4 c) { f32x4 d = __builtin_amdgcn_mfma_f32_16x16x32_bf16(a, b, c, 0, 0, 0); asm volatile("" :: "v"(d)); return d; }
DI f32x16 mfmaL(bf16x8 a, bf16x8 b, f32x16 c) { f32x16 d = __builtin_amdgcn_mfma_f32_32x32x16_bf16(a, b, c, 0, 0, 0); asm volatile("" :: "v"(d)); return d; }
DI f32x4 mfma16(s16x4 a, s16x4 b, f32x4 c) { f32x4 d = __builtin_amdgcn_mfma_f32_16x16x16bf16_1k(a, b, c, 0, 0, 0); asm volatile("" :: "v"(d)); return d; }
DI unsigned pk2(float lo, float hi) { f32x2 v = {lo, hi}; return __builtin_bit_cast(unsigned, __builtin_convertvector(v, bf16x2)); }
DI s16x4 tr_read(lds_char_p p) { return __builtin_amdgcn_ds_read_tr16_b64_v4i16((lds_s16x4_p)p); }
DI bf16x8 tr_frag(lds_char_p p, int second) {
  s16x4 lo = tr_read(p), hi = tr_read(p + second);
  return __builtin_bit_cast(bf16x8, __builtin_shufflevector(lo, hi, 0, 1, 2, 3, 4, 5, 6, 7));
}
DI bf16x8 pack8(f32x4 a, f32x4 b) {
  u32x4 w = {pk2(a[0], a[1]), pk2(a[2], a[3]), pk2(b[0], b[1]), pk2(b[2], b[3])};
  return __builtin_bit_cast(bf16x8, w);
}
DI float max3(float a, float b, float c) { return __builtin_fmaxf(__builtin_fmaxf(a, b), c); }
DI float xh_max(float x) {
  auto a = __builtin_amdgcn_permlane32_swap(__float_as_uint(x), __float_as_uint(x), false, false);
  return fmaxf(__uint_as_float(a[0]), __uint_as_float(a[1]));
}
DI float xh_sum(float x) {
  auto a = __builtin_amdgcn_permlane32_swap(__float_as_uint(x), __float_as_uint(x), false, false);
  return __uint_as_float(a[0]) + __uint_as_float(a[1]);
}
template <int B> DI bf16x8 pack8s(const f32x16& v, float k) {
  u32x4 w = {pk2(v[B] * k, v[B + 1] * k), pk2(v[B + 2] * k, v[B + 3] * k), pk2(v[B + 4] * k, v[B + 5] * k), pk2(v[B + 6] * k, v[B + 7] * k)};
  return __builtin_bit_cast(bf16x8, w);
}
DI float max16(float m, const f32x16& v) {
#pragma unroll
  for (int e = 0; e < 16; e += 2) m = max3(m, v[e], v[e + 1]);
  return m;
}
DI float xg_max(float x) {
  auto a = __builtin_amdgcn_permlane32_swap(__float_as_uint(x), __float_as_uint(x), false, false);
  x = fmaxf(__uint_as_float(a[0]), __uint_as_float(a[1]));
  auto b = __builtin_amdgcn_permlane16_swap(__float_as_uint(x), __float_as_uint(x), false, false);
  return fmaxf(__uint_as_float(b[0]), __uint_as_float(b[1]));
}
DI float xg_sum(float x) {
  auto a = __builtin_amdgcn_permlane32_swap(__float_as_uint(x), __float_as_uint(x), false, false);
  x = __uint_as_float(a[0]) + __uint_as_float(a[1]);
  auto b = __builtin_amdgcn_permlane16_swap(__float_as_uint(x), __float_as_uint(x), false, false);
  return __uint_as_float(b[0]) + __uint_as_float(b[1]);
}

#define DMA_HEAD "s_mov_b32 %0, m0\n\ts_mov_b32 m0, %1\n\ts_nop 0\n\t"
#define DMA_TAIL "s_mov_b32 m0, %0"
DI void dma4(unsigned lds, const char* base, unsigned v0, unsigned v1, unsigned v2, unsigned v3) {
  unsigned keep;
  lds = __builtin_amdgcn_readfirstlane(lds);
  asm volatile(DMA_HEAD
      "global_load_lds_dwordx4 %3, %2\n\t"
      "global_load_lds_dwordx4 %4, %2 offset:1024\n\t"
      "global_load_lds_dwordx4 %5, %2 offset:2048\n\t"
      "global_load_lds_dwordx4 %6, %2 offset:3072\n\t" DMA_TAIL
      : "=&s"(keep) : "s"(lds), "s"(base), "v"(v0), "v"(v1), "v"(v2), "v"(v3) : "memory");
}
template <int OFF, bool NT> DI void dma1(unsigned lds, const char* base, unsigned v) {
  lds = __builtin_amdgcn_readfirstlane(lds);
  if constexpr (NT) asm volatile("s_mov_b32 m0, %0\n\ts_nop 0\n\tglobal_load_lds_dwordx4 %2, %1 offset:%3 nt" :: "s"(lds), "s"(base), "v"(v), "i"(OFF) : "memory");
  else              asm volatile("s_mov_b32 m0, %0\n\ts_nop 0\n\tglobal_load_lds_dwordx4 %2, %1 offset:%3" :: "s"(lds), "s"(base), "v"(v), "i"(OFF) : "memory");
}
template <bool NT> DI void touch(unsigned& dst, const char* base, unsigned voff) {
  if constexpr (NT) asm volatile("global_load_dword %0, %1, %2 nt" : "+v"(dst) : "v"(voff), "s"(base) : "memory");
  else              asm volatile("global_load_dword %0, %1, %2" : "+v"(dst) : "v"(voff), "s"(base) : "memory");
}
DI void keep(unsigned x) { asm volatile("" :: "v"(x)); }
DI void opaque(unsigned& x) { asm volatile("" : "+v"(x)); }
DI void dma_quarter(unsigned lds, const char* base, unsigned v) {
  lds = __builtin_amdgcn_readfirstlane(lds);
  asm volatile("s_mov_b32 m0, %0\n\ts_mov_b64 exec, 0xffff\n\tglobal_load_lds_dwordx4 %2, %1\n\ts_mov_b64 exec, -1" :: "s"(lds), "s"(base), "v"(v) : "memory");
}
template <int OFF> DI void gload16(bf16x8& dst, const char* base, unsigned voff) {
  asm volatile("global_load_dwordx4 %0, %1, %2 offset:%3" : "=v"(dst) : "v"(voff), "s"(base), "i"(OFF) : "memory");
}
#define WAIT_VM(n) asm volatile("s_waitcnt vmcnt(" #n ")" ::: "memory")
#define MEM_FENCE() asm volatile("" ::: "memory")
#define WAIT_LGKM(n) do { MEM_FENCE(); __builtin_amdgcn_s_waitcnt(0xC07F | ((n) << 8)); MEM_FENCE(); } while (0)
#define BARRIER() do { WAIT_LGKM(0); __builtin_amdgcn_s_barrier(); MEM_FENCE(); } while (0)
#define SCHED_FENCE() do { MEM_FENCE(); __builtin_amdgcn_sched_barrier(0); } while (0)

#define STAMP(i)
#define TICK(i)
#define DIAG_PARAMS
#define DIAG_ARGS
#define DIAG_DECL(n)
#define DIAG_FLUSH(n, ptr, slot)

DI bf16x8 q_frag(f32x4 t0, f32x4 t1) {
  unsigned a0 = pk2(t0[0] * QSCALE, t0[1] * QSCALE), a1 = pk2(t0[2] * QSCALE, t0[3] * QSCALE);
  unsigned b0 = pk2(t1[0] * QSCALE, t1[1] * QSCALE), b1 = pk2(t1[2] * QSCALE, t1[3] * QSCALE);
  auto p0 = __builtin_amdgcn_permlane32_swap(a0, b0, false, false);
  auto p1 = __builtin_amdgcn_permlane32_swap(a1, b1, false, false);
  auto r0 = __builtin_amdgcn_permlane16_swap(p0[0], p0[1], false, false);
  auto r1 = __builtin_amdgcn_permlane16_swap(p1[0], p1[1], false, false);
  u32x4 w = {r0[0], r1[0], r0[1], r1[1]};
  return __builtin_bit_cast(bf16x8, w);
}

DI void ostore(char* p, bf16x8 v) {
  *(bf16x8*)p = v;
}
DI lds_char_p lds_at(unsigned a) { return (lds_char_p)(uintptr_t)a; }
#define LDS_RD(T, p) (*(__attribute__((address_space(3))) T*)(p))
template <int N> DI void wait_vm() { asm volatile("s_waitcnt vmcnt(%0)" :: "i"(N) : "memory"); }

struct AttnState {
  bf16x8 qf[8], q9[4];
  f32x16 o[4];
  f32x4 o9[8];
  f32x16 c;
  float c9;
  float ls, ls9;
  f32x16 sE, sO;
  f32x4 s9;
  u32x4 pE[2];
  bf16x8 kc[4];
  unsigned rdk, rdk9, rdv, rdv9, vk0, vv0, ldsw;
  const char *kg, *vg;
  int rk;
};
#define EXP2X(v, e, dst, l) do { const float x0_ = __builtin_amdgcn_exp2f(v[e]); float x1_ = __builtin_amdgcn_exp2f(v[(e) + 1]); \
    dst = pk2(x0_, x1_); l += x0_; asm volatile("" : "+v"(l), "+v"(dst), "+v"(x1_)); v[(e) + 1] = x1_; } while (0)
#define ADD1(l, x) do { l += (x); asm volatile("" : "+v"(l)); } while (0)
#define MAX2X(m, v, e) do { m = max3(m, v[e], v[(e) + 1]); asm volatile("" : "+v"(m)); } while (0)
#define BF8(x) __builtin_bit_cast(bf16x8, x)
#define K_RD(kb, s) LDS_RD(bf16x8, (((s) & 1) ? ko : ke) + 8192 * (kb) + 512 * ((s) >> 1))
#define V_RD(kb, f) tr_frag(vr + 8192 * (kb) + 4096 * ((f) >> 2) + 512 * ((f) & 3), 2048)
template <int I> DI void dma_k(const AttnState& A, int slot, int t) {
  dma1<128 * (I & 1) + 2048 * (I >> 1), true>(A.ldsw + slot * SLOT_B + 896 * (I & 1), A.kg + ((t + A.rk) & 7) * TILE_B, I < 2 ? A.vk0 : (A.vk0 ^ 32));
}
template <int I> DI void dma_v(const AttnState& A, int slot, int t) {
  dma1<128 * (I & 1) + 2048 * (I >> 1), true>(A.ldsw + slot * SLOT_B + 896 * (I & 1), A.vg + ((t + A.rk) & 7) * TILE_B, A.vv0);
}
DI void dma_k4(const AttnState& A, int slot, int t) { dma_k<0>(A, slot, t); dma_k<1>(A, slot, t); dma_k<2>(A, slot, t); dma_k<3>(A, slot, t); }
DI void dma_v4(const AttnState& A, int slot, int t) { dma_v<0>(A, slot, t); dma_v<1>(A, slot, t); dma_v<2>(A, slot, t); dma_v<3>(A, slot, t); }
template <int I> DI void dma_kv(const AttnState& A, int slotk, int slotv, int t) {
  if constexpr (I < 4) dma_k<I>(A, slotk, t); else dma_v<I - 4>(A, slotv, t);
}
DI void rescale_own(AttnState& A, f32x16& s) {
  const float d = xh_max(max16(0.f, s)), a = __builtin_amdgcn_exp2f(-d);
  A.c -= d; s -= d; A.ls *= a;
#pragma unroll
  for (int i = 0; i < 4; ++i) A.o[i] *= a;
}
DI void rescale_shared(AttnState& A) {
  const float d = xg_max(max3(max3(0.f, A.s9[0], A.s9[1]), A.s9[2], A.s9[3])), a = __builtin_amdgcn_exp2f(-d);
  A.c9 -= d; A.s9 -= d; A.ls9 *= a;
#pragma unroll
  for (int i = 0; i < 8; ++i) A.o9[i] *= a;
}
DI void attn_x2a(AttnState& A, lds_char_p ke, lds_char_p ko, bf16x8* k2) {
  FOR_I(4)
    A.sO = mfmaL(k2[i], A.qf[i], i ? A.sO : A.c);
    A.kc[i] = K_RD(1, 4 + i);
    EXP2X(A.sE, 2 * i, A.pE[0][i], A.ls);
    SCHED_FENCE();
  END_FOR
}
template <int MODE> DI void attn_step(AttnState& A, int t, int sk DIAG_PARAMS) {
  constexpr bool LAST = MODE == 2;
  const int sv = sk == 4 ? 0 : sk + 1, sn = sv == 4 ? 0 : sv + 1, sp = sk == 0 ? 4 : sk - 1;
  TICK(0);
  if (LAST || t == 0) wait_vm<0>(); else wait_vm<4>();
  TICK(1);
  BARRIER();
  TICK(2);
  PRIO(3);
  lds_char_p vr = lds_at(A.rdv + sv * SLOT_B);
  bf16x8 va[8], vb[8], k0[8], k2[4], k9[4];
  s16x4 v9[8];
  u32x4 pO[2];
  u32x2 p9;
  float mx;
  bool grow;
  FOR_I(4)
    A.sO = mfmaL(A.kc[i], A.qf[4 + i], A.sO);
    if constexpr (i < 2) { va[2 * i] = V_RD(0, 2 * i); va[2 * i + 1] = V_RD(0, 2 * i + 1); }
    else va[2 + i] = V_RD(0, 2 + i);
    EXP2X(A.sE, 8 + 2 * i, A.pE[1][i], A.ls);
    SCHED_FENCE();
  END_FOR
  lds_char_p ve9 = lds_at(A.rdv9 + sv * SLOT_B), vo9 = lds_at((A.rdv9 + sv * SLOT_B) ^ 32);
  lds_char_p ke = lds_at(A.rdk + sn * SLOT_B), ko = lds_at((A.rdk + sn * SLOT_B) ^ 32);
  FOR_I(8)
    A.o[i & 3] = mfmaL(va[i], BF8(A.pE[i >> 2]), A.o[i & 3]);
    if constexpr (i < 2) va[6 + i] = V_RD(0, 6 + i);
    else if constexpr (i < 6) { v9[2 * (i - 2)] = tr_read(ve9 + 512 * (i - 2)); v9[2 * (i - 2) + 1] = tr_read(vo9 + 512 * (i - 2)); }
    else if constexpr (!LAST) k0[i - 6] = K_RD(0, i - 6);
    if constexpr (MODE == 0) dma_kv<i>(A, sp, sk, t + 2);
    ADD1(A.ls, A.sE[2 * i + 1]);
    if constexpr (i == 7) PRIO(2);
    if constexpr (i == 0) { mx = 0.f; EXP2X(A.s9, 0, p9[0], A.ls9); }
    if constexpr (i == 1) EXP2X(A.s9, 2, p9[1], A.ls9);
    if constexpr (i >= 2) MAX2X(mx, A.sO, 2 * (i - 2));
    SCHED_FENCE();
  END_FOR
  FOR_I(4)
    A.o9[2 * i] = mfma16(v9[2 * i], __builtin_bit_cast(s16x4, p9), A.o9[2 * i]);
    if constexpr (LAST) { vb[2 * i] = V_RD(1, 2 * i); vb[2 * i + 1] = V_RD(1, 2 * i + 1); }
    else k0[2 + i] = K_RD(0, 2 + i);
    A.o9[2 * i + 1] = mfma16(v9[2 * i + 1], __builtin_bit_cast(s16x4, p9), A.o9[2 * i + 1]);
    if constexpr (i < 2) { MAX2X(mx, A.sO, 12 + 2 * i); ADD1(A.ls9, A.s9[2 * i + 1]); }
    if constexpr (i == 2) grow = __any(mx > RESCALE_THR);
    SCHED_FENCE();
  END_FOR
  if (__builtin_expect(grow, 0)) rescale_own(A, A.sO);
  SCHED_FENCE();
  lds_char_p kn9 = lds_at(A.rdk9 + sn * SLOT_B);
  FOR_I(8)
    if constexpr (!LAST) {
      A.sE = mfmaL(k0[i], A.qf[i], i ? A.sE : A.c);
      if constexpr (i < 2) k0[6 + i] = K_RD(0, 6 + i);
      else if constexpr (i < 6) k9[i - 2] = LDS_RD(bf16x8, kn9 + 512 * (i - 2));
      else vb[i - 6] = V_RD(1, i - 6);
    }
    EXP2X(A.sO, 2 * i, pO[i >> 2][i & 3], A.ls);
    if constexpr (i == 6) PRIO(1);
    SCHED_FENCE();
  END_FOR
  if constexpr (!LAST) {
    FOR_I(2)
      A.s9 = mfma32(k9[2 * i], A.q9[2 * i], i ? A.s9 : f32x4{A.c9, A.c9, A.c9, A.c9});
      vb[2 + 2 * i] = V_RD(1, 2 + 2 * i);
      A.s9 = mfma32(k9[2 * i + 1], A.q9[2 * i + 1], A.s9);
      vb[3 + 2 * i] = V_RD(1, 3 + 2 * i);
      SCHED_FENCE();
    END_FOR
  }
  FOR_I(8)
    A.o[i & 3] = mfmaL(vb[i], BF8(pO[i >> 2]), A.o[i & 3]);
    if constexpr (!LAST) {
      if constexpr (i < 2) vb[6 + i] = V_RD(1, 6 + i);
      else if constexpr (i < 6) k2[i - 2] = K_RD(1, i - 2);
    }
    ADD1(A.ls, A.sO[2 * i + 1]);
    if constexpr (i == 6) PRIO(0);
    if constexpr (!LAST) {
      if constexpr (i == 0) mx = 0.f;
      if constexpr (i < 4) { MAX2X(mx, A.sE, 4 * i); MAX2X(mx, A.sE, 4 * i + 2); }
      if constexpr (i == 4 || i == 5) MAX2X(mx, A.s9, 2 * (i - 4));
      if constexpr (i == 6) grow = __any(mx > RESCALE_THR);
    }
    SCHED_FENCE();
  END_FOR
  if constexpr (!LAST) {
    if (__builtin_expect(grow, 0)) { rescale_own(A, A.sE); rescale_shared(A); }
    SCHED_FENCE();
    attn_x2a(A, ke, ko, k2);
  }
}

constexpr int qp_issued(int s) { return (s <= 9 ? 2 : 0) + ((s & 1) && s <= 9 ? 3 : 0) + (s == 11 || s == 15 ? 4 : s == 13 ? 8 : 0); }
constexpr int qp_cum(int s) { int n = 21; for (int j = 0; j < s; ++j) n += qp_issued(j); return n; }
constexpr int qp_endw(int i) { return i <= 5 ? 7 * (i >> 1) + 5 + 2 * (i & 1) : qp_cum(i - 6) + 2; }
constexpr int qp_endx(int c) { return c <= 2 ? 7 * c + 3 : qp_cum(2 * c - 5) + 5; }
constexpr int qp_wait(int s) { const int a = qp_endw(s + 1), b = qp_endx((s + 1) >> 1); return qp_cum(s) - (a > b ? a : b); }

__global__ __launch_bounds__(512, 2)
void qattn_kernel(const char* __restrict__ xA, const char* __restrict__ kA, const char* __restrict__ vA, const char* __restrict__ wqA,
                  const char* __restrict__ xB, const char* __restrict__ kB, const char* __restrict__ vB, const char* __restrict__ wqB,
                  char* __restrict__ ows) {
  extern __shared__ __attribute__((aligned(1024))) char smem_raw[];
  const int tid = threadIdx.x, lane = tid & 63, r = lane & 15, g = lane >> 4, r32 = lane & 31, hh = lane >> 5;
  const int wv = __builtin_amdgcn_readfirstlane(tid >> 6), w = wv & 3, gq = wv >> 2;
  const unsigned ldsb = (unsigned)(uintptr_t)(lds_char_p)smem_raw, lds0 = ldsb + gq * TILE_B;
  const int bid = blockIdx.x, xcd = bid & 7, idx = bid >> 3;
  const int br = xcd >> 2, h = 8 * (xcd & 3) + 2 * (idx & 3) + gq, b = idx >> 2;
  const int rot = ROT ? (b + 2 * (idx & 3)) & 7 : 0;
  const char* xg = (br ? xB : xA) + (size_t)b * SQ * DM * 2;
  const char* wg = (br ? wqB : wqA) + h * DH * 2;
  const int q4 = (lane >> 2) & 3, p4 = lane & 3, r8 = (lane >> 2) & 7, blk = g & 1;

  AttnState A;
  A.kg = (br ? kB : kA) + (size_t)(b * NH + h) * SK * DH * 2;
  A.vg = (br ? vB : vA) + (size_t)(b * NH + h) * SK * DH * 2;
  A.rk = ROTK ? (h + b) & 7 : 0;
  A.ldsw = lds0 + w * 4096;
  A.vk0 = (16 * w + r8) * 256 + 64 * hh + 16 * (p4 ^ blk);
  A.vv0 = (16 * w + r8) * 256 + 64 * hh + 16 * (p4 ^ (2 * blk));
  DIAG_DECL(6);
  STAMP(0);
  {
    constexpr int XOFF = 3 * SLOT_B, XCH = SQ * 128, EXOFF = XOFF + 3 * XCH;
    const unsigned vw0 = (8 * w + r8) * (HD * 2) + 64 * hh + 16 * (p4 ^ (2 * (w & 1)));
#define WPIECE(s) (((((s) + 2) % 6) >> 1) * SLOT_B + ((s) & 1) * 8192)
#define DMA_W(s, i) dma1<128 * (i), W_NT>(lds0 + WPIECE(s) + w * 2048 + 896 * (i), wg + (size_t)(((s) + 2 * rot) & 15) * 32 * HD * 2, vw0)
    unsigned vx[3];
    FOR_I(3) const int row = 18 * wv + 8 * i + (i < 2 ? lane >> 3 : (lane >> 3) & 1);
             vx[i] = row * (DM * 2) + 16 * ((lane & 7) ^ ((row >> 1) & 7)); END_FOR
#define XPOS(c) ((((c) + 1) % 3) * XCH)
#define DMA_X(c, i) do { const unsigned l_ = ldsb + XOFF + XPOS(c) + wv * (18 * 128) + 1024 * (i); const char* s_ = xg + 128 * (((c) + rot) & 7); \
      if constexpr ((i) < 2) dma1<0, X_NT>(l_, s_, vx[i]); else dma_quarter(l_, s_, vx[2]); } while (0)
    unsigned xr[6];
    FOR_I(4) xr[i] = (ldsb + XOFF + (32 * w + r32) * 128 + 16 * (hh ^ ((r32 >> 1) & 7))) ^ (32 * i); END_FOR
    FOR_I(2) xr[4 + i] = (ldsb + XOFF + (128 + r) * 128 + 16 * (g ^ (r >> 1))) ^ (64 * i); END_FOR
#define X_RD(set, s, i) xf[set][i] = LDS_RD(bf16x8, lds_at(xr[(i) < 2 ? 2 * ((s) & 1) + (i) : 4 + ((s) & 1)]) + XPOS((s) >> 1))
    unsigned rw[6];
    rw[0] = lds0 + 2048 * hh + 64 * q4 + 32 * (blk ^ hh) + 8 * (((p4 & 1) << 1) | (p4 >> 1));
    rw[1] = lds0 + 2048 * g + 64 * q4 + 32 * blk + 8 * p4 + 512 * w; rw[2] = rw[1] ^ 32;
    FOR_I(3) rw[3 + i] = rw[i] + 2 * SLOT_B; END_FOR
    FOR_I(6) opaque(rw[i]); opaque(xr[i]); END_FOR
#define W_RD(set, s, i) wf[set][i] = tr_frag(lds_at(rw[((i) < 8 ? 0 : 1 + ((i) & 1)) + (WPIECE(s) >= 2 * SLOT_B ? 3 : 0)]) + \
                                             (((i) < 8 ? 4096 * (((i) >> 2) & 1) + 512 * ((i) & 3) : 0) + WPIECE(s) % (2 * SLOT_B)), 256)
    bf16x8 xf[2][3], wf[2][10];
    f32x16 acc[4];
    f32x4 acc9[2];
#pragma unroll
    for (int i = 0; i < 4; ++i) acc[i] = f32x16{};
    acc9[0] = f32x4{0, 0, 0, 0}; acc9[1] = f32x4{0, 0, 0, 0};

    FOR_I(3)
      DMA_X(i, 0); DMA_X(i, 1); DMA_X(i, 2);
      DMA_W(2 * i, 0); DMA_W(2 * i, 1); DMA_W(2 * i + 1, 0); DMA_W(2 * i + 1, 1);
    END_FOR
    wait_vm<16>();
    BARRIER();
    STAMP(1);
    FOR_I(3) X_RD(0, 0, i); END_FOR
    FOR_I(10) W_RD(0, 0, i); END_FOR
    static_for<16>([&](auto sc_) __attribute__((always_inline)) {
      constexpr int s = decltype(sc_)::value;
      constexpr int cs = s & 1, ns = cs ^ 1;
      if constexpr (s <= 14) wait_vm<qp_wait(s)>();
      BARRIER();
      if constexpr (s == 8) STAMP(2);
      SCHED_FENCE();
      FOR_I(9)
        if constexpr (i == 0) PRIO(3);
        if constexpr (i == 3) PRIO(2);
        if constexpr (i == 5) PRIO(1);
        if constexpr (i == 7) PRIO(0);
        if constexpr (i < 8) acc[i & 3] = mfmaL(wf[cs][i], xf[cs][i >> 2], acc[i & 3]);
        else { acc9[0] = mfma32(wf[cs][8], xf[cs][2], acc9[0]); acc9[1] = mfma32(wf[cs][9], xf[cs][2], acc9[1]); }
        SCHED_FENCE();
        if constexpr (s < 15) {
          if constexpr (i < 7) W_RD(ns, s + 1, i);
          if constexpr (i < 6) { if constexpr (i & 1) W_RD(ns, s + 1, 7 + (i >> 1)); else X_RD(ns, s + 1, i >> 1); }
        }
        if constexpr (s <= 9 && (i == 1 || i == 3)) DMA_W(s + 6, i >> 1);
        if constexpr ((s & 1) && s <= 9 && i >= 5 && i < 8) DMA_X((s + 5) >> 1, i - 5);
        if constexpr (s == 11 && (i & 1)) dma_k<(i >> 1)>(A, 0, 0);
        if constexpr (s == 13 && i < 8) dma_v<(i & 3)>(A, i < 4 ? 1 : 3, i >> 2);
        if constexpr (s == 15 && (i & 1)) dma_k<(i >> 1)>(A, 2, 1);
        SCHED_FENCE();
      END_FOR
    });
    lds_char_p ex = lds_at(ldsb + EXOFF + gq * 4096 + lane * 16);
    LDS_RD(bf16x8, ex + w * 1024) = q_frag(acc9[0], acc9[1]);
    FOR_I(8) A.qf[i] = pack8s<8 * (i & 1)>(acc[i >> 1], QSCALE); END_FOR
    wait_vm<12>();
    BARRIER();
    FOR_I(4) A.q9[i] = LDS_RD(bf16x8, ex + i * 1024); END_FOR
#undef WPIECE
#undef DMA_W
#undef XPOS
#undef DMA_X
#undef X_RD
#undef W_RD
  }

  STAMP(3);
#pragma unroll
  for (int i = 0; i < 4; ++i) A.o[i] = f32x16{};
#pragma unroll
  for (int i = 0; i < 8; ++i) A.o9[i] = f32x4{0, 0, 0, 0};
  A.rdk = lds0 + 2048 * (r32 >> 3) + 64 * (r32 & 7) + 16 * (hh ^ ((r32 >> 2) & 3));
  A.rdk9 = lds0 + 4096 * w + 2048 * (r >> 3) + 64 * (r & 7) + 16 * (g ^ (r >> 2));
  A.rdv = lds0 + 64 * (4 * hh + q4) + 32 * (blk ^ hh) + 8 * p4;
  A.rdv9 = lds0 + 4096 * w + 2048 * hh + 64 * (4 * blk + q4) + 32 * blk + 8 * p4;
  {
    lds_char_p ke = lds_at(A.rdk), ko = lds_at(A.rdk ^ 32);
    bf16x8 k0[8], k2[4], k9[4];
    A.ls = 0.f; A.ls9 = 0.f;
    FOR_I(8) k0[i] = K_RD(0, i); END_FOR
    FOR_I(4) k9[i] = LDS_RD(bf16x8, lds_at(A.rdk9) + 512 * i); k2[i] = K_RD(1, i); END_FOR
    A.sE = f32x16{}; A.s9 = f32x4{0, 0, 0, 0};
    FOR_I(8) A.sE = mfmaL(k0[i], A.qf[i], A.sE); END_FOR
    FOR_I(4) A.s9 = mfma32(k9[i], A.q9[i], A.s9); END_FOR
    const float m = xh_max(max16(A.sE[0], A.sE));
    A.c = f32x16{} - m; A.sE -= m;
    const float m9 = xg_max(fmaxf(max3(A.s9[0], A.s9[1], A.s9[2]), A.s9[3]));
    A.c9 = -m9; A.s9 -= m9;
    SCHED_FENCE();
    attn_x2a(A, ke, ko, k2);
  }
  TICK(7);
  int sk = 0;
#pragma unroll 1
  for (int t = 0; t < 6; ++t) {
    asm volatile("" : "+v"(A.vk0), "+s"(sk));
    attn_step<0>(A, t, sk DIAG_ARGS);
    sk = sk >= 3 ? sk - 3 : sk + 2;
  }
  attn_step<1>(A, 6, sk DIAG_ARGS);
  attn_step<2>(A, 7, sk + 2 DIAG_ARGS);
  TICK(0);

  STAMP(4);
  int ln;
  asm volatile("v_mbcnt_lo_u32_b32 %0, -1, 0\n\tv_mbcnt_hi_u32_b32 %0, -1, %0" : "=v"(ln));
  const int lr = ln & 15, lg = ln >> 4;
  char* ob = ows + ((size_t)((br * NB + b) * NQT) * NH + h) * 4096;
  lds_char_p xo = lds_at(ldsb + SLOT_B + gq * SLOT_B);
  lds_char_p xst = lds_at(lds0 + 3 * SLOT_B);
  {
    const float l9 = xg_sum(A.ls9);
#pragma unroll
    for (int dt = 0; dt < 8; ++dt) LDS_RD(f32x4, xo + (w * 8 + dt) * 1024 + ln * 16) = A.o9[dt];
    LDS_RD(f32x2, xst + w * 512 + ln * 8) = f32x2{A.c9, l9};
  }
  {
    const float inv = 1.0f / xh_sum(A.ls);
    char* dst = ob + (size_t)(2 * w + (lg & 1)) * NH * 4096 + (lg >> 1) * 256 + lr * 16;
    FOR_I(8) ostore(dst + (i >> 1) * 1024 + (i & 1) * 512, pack8s<8 * (i & 1)>(A.o[i >> 1], inv)); END_FOR
  }
  {
    BARRIER();
    f32x2 st[4];
#pragma unroll
    for (int j = 0; j < 4; ++j) st[j] = LDS_RD(f32x2, xst + j * 512 + ln * 8);
    const float nmin = fminf(fminf(st[0][0], st[1][0]), fminf(st[2][0], st[3][0]));
    float f[4], l = 0.f;
#pragma unroll
    for (int j = 0; j < 4; ++j) { f[j] = __builtin_amdgcn_exp2f(nmin - st[j][0]); l += f[j] * st[j][1]; }
    const float inv = 1.0f / l;
    f32x4 t0 = {0, 0, 0, 0}, t1 = {0, 0, 0, 0};
    lds_char_p src = xo + (2 * w + (lg >> 1)) * 1024 + (16 * (lg & 1) + lr) * 16;
#pragma unroll
    for (int j = 0; j < 4; ++j) {
      t0 += LDS_RD(f32x4, src + j * 8192) * (f[j] * inv);
      t1 += LDS_RD(f32x4, src + j * 8192 + 512) * (f[j] * inv);
    }
    ostore(ob + (size_t)8 * NH * 4096 + w * 1024 + ln * 16, pack8(t0, t1));
  }
  STAMP(5);
}

template <int NQ> struct OSet { bf16x8 f[NQ]; };
template <int NQ> DI void o_issue(OSet<NQ>& s, const char* ob, unsigned vo) {
#pragma unroll
  for (int i = 0; i < NQ; ++i) gload16<0>(s.f[i], ob + (size_t)i * NH * 4096, vo);
}
template <int NQ> DI void o_pin(OSet<NQ>& s) {
#pragma unroll
  for (int i = 0; i < NQ; ++i) asm volatile("" : "+v"(s.f[i]));
}

template <int NQ>
DI void oproj_body(lds_char_p smem, const char* ob, const char* wo, char* outp, int w, int tid, const char* dbg) {
  DIAG_DECL(6);
  STAMP(0);
  const int lane = tid & 63, g = lane >> 4, q4 = (lane >> 2) & 3, p4 = lane & 3;
  const unsigned lds0 = (unsigned)(uintptr_t)smem + w * 20480;
  lds_char_p my = smem + w * 20480;
  const int drow = lane >> 3;
  const unsigned vw0 = drow * (DM * 2) + 16 * ((lane & 7) ^ (((drow >> 1) & 3) << 1));
  constexpr unsigned WSTEP = 8 * DM * 2 - 1024;
  const unsigned vo = lane * 16;
  const int rrow = 16 * (g >> 1) + 4 * (g & 1) + q4;
  const unsigned rd = rrow * 128 + 32 * ((rrow >> 1) & 3) + 8 * p4;
  const char* wk = wo + (size_t)(16 * w) * 32 * DM * 2;
  const char* ok = ob + (16 * w) * 1024;
  constexpr int PER = NQ + 4;
  OSet<NQ> os[4];
  f32x4 acc[NQ][4];
#pragma unroll
  for (int i = 0; i < NQ; ++i)
#pragma unroll
    for (int n = 0; n < 4; ++n) acc[i][n] = f32x4{0, 0, 0, 0};
#pragma unroll
  for (int i = 0; i < 4; ++i) {
    dma4(lds0 + i * 4096, wk + (size_t)i * 32 * DM * 2, vw0, vw0 + WSTEP, vw0 + 2 * WSTEP, vw0 + 3 * WSTEP);
    o_issue<NQ>(os[i], ok + i * 1024, vo);
  }
#pragma unroll
  for (int i = 0; i < 16; ++i) {
    if (i <= 12) wait_vm<3 * PER>(); else if (i == 13) wait_vm<2 * PER>(); else if (i == 14) wait_vm<PER>(); else wait_vm<0>();
    o_pin<NQ>(os[i & 3]);
    if (i == 0) STAMP(1);
    if (i == 4) STAMP(2);
    if (i == 10) STAMP(3);
#pragma unroll
    for (int n = 0; n < 4; ++n) {
      bf16x8 a = tr_frag(my + ((rd ^ (32 * n)) + (i & 3) * 4096), 1024);
#pragma unroll
      for (int j = 0; j < NQ; ++j) acc[j][n] = mfma32(a, os[i & 3].f[j], acc[j][n]);
    }
    SCHED_FENCE();
    if (i + 4 < 16) {
      WAIT_LGKM(0);
      dma4(lds0 + (i & 3) * 4096, wk + (size_t)(i + 4) * 32 * DM * 2, vw0, vw0 + WSTEP, vw0 + 2 * WSTEP, vw0 + 3 * WSTEP);
      o_issue<NQ>(os[i & 3], ok + (i + 4) * 1024, vo);
    }
    SCHED_FENCE();
  }
  STAMP(4);
#pragma unroll
  for (int j = 0; j < NQ; ++j)
#pragma unroll
    for (int n = 0; n < 4; ++n) *(__attribute__((address_space(3))) f32x4*)(my + (j * 4 + n) * 1024 + lane * 16) = acc[j][n];
  BARRIER();
#pragma unroll
  for (int it = 0; it < 3; ++it) {
    const int tile = it * 8 + w;
    if (tile < NQ * 4) {
      f32x4 sum = {0, 0, 0, 0};
#pragma unroll
      for (int j = 0; j < 8; ++j) sum += *(__attribute__((address_space(3))) f32x4*)(smem + j * 20480 + tile * 1024 + lane * 16);
      const int qt = tile >> 2, n = tile & 3;
      u32x2 u = {pk2(sum[0], sum[1]), pk2(sum[2], sum[3])};
      *(u32x2*)(outp + (size_t)(16 * qt + (lane & 15)) * (2 * DM * 2) + (16 * n + 4 * g) * 2) = u;
    }
  }
  STAMP(5);
}

__global__ __launch_bounds__(512, 2)
void oproj_kernel(const char* __restrict__ ows, const char* __restrict__ woA, const char* __restrict__ woB, char* __restrict__ out) {
  extern __shared__ __attribute__((aligned(16))) char smem_raw[];
  lds_char_p smem = (lds_char_p)smem_raw;
  const int tid = threadIdx.x;
  const int w = __builtin_amdgcn_readfirstlane(tid >> 6);
  const int bid = blockIdx.x, xcd = bid & 7, idx = bid >> 3;
  const int br = xcd >> 2, b = 2 * (xcd & 3) + (idx & 1), rg = (idx >> 1) & 1, cg = idx >> 2;
  const int qt0 = rg ? 5 : 0;
  const char* ob = ows + (size_t)((br * NB + b) * NQT + qt0) * NH * 4096;
  const char* wo = (br ? woB : woA) + cg * 128;
  char* outp = out + ((size_t)(b * SQ + 16 * qt0) * (2 * DM) + br * DM + cg * 64) * 2;
  if (rg == 0) oproj_body<5>(smem, ob, wo, outp, w, tid, ows);
  else         oproj_body<4>(smem, ob, wo, outp, w, tid, ows);
}

extern "C" void kernel_launch(void* const* d_in, const int* in_sizes, int n_in,
                              void* d_out, int out_size, void* d_ws, size_t ws_size,
                              hipStream_t stream) {
  (void)in_sizes; (void)n_in; (void)out_size; (void)ws_size;
  (void)hipFuncSetAttribute((const void*)qattn_kernel, hipFuncAttributeMaxDynamicSharedMemorySize, LDS1);
  (void)hipFuncSetAttribute((const void*)oproj_kernel, hipFuncAttributeMaxDynamicSharedMemorySize, LDS2);
  hipLaunchKernelGGL(qattn_kernel, dim3(GRID1), dim3(512), LDS1, stream,
                     (const char*)d_in[0], (const char*)d_in[1], (const char*)d_in[2], (const char*)d_in[3],
                     (const char*)d_in[5], (const char*)d_in[6], (const char*)d_in[7], (const char*)d_in[8], (char*)d_ws);
  hipLaunchKernelGGL(oproj_kernel, dim3(256), dim3(512), LDS2, stream,
                     (const char*)d_ws, (const char*)d_in[4], (const char*)d_in[9], (char*)d_out);
}
```

```cpp
#define DIAG 1
#define STEPS 1
#include <stdio.h>
#include <vector>
#include <algorithm>
#include <hip/hip_runtime.h>
#include <hip/hip_bf16.h>
#include <stdint.h>
#include <utility>

typedef short    s16x4  __attribute__((ext_vector_type(4)));
typedef short    s16x8  __attribute__((ext_vector_type(8)));
typedef __bf16   bf16x2 __attribute__((ext_vector_type(2)));
typedef __bf16   bf16x8 __attribute__((ext_vector_type(8)));
typedef float    f32x2  __attribute__((ext_vector_type(2)));
typedef float    f32x4  __attribute__((ext_vector_type(4)));
typedef float    f32x16 __attribute__((ext_vector_type(16)));
typedef unsigned u32x2  __attribute__((ext_vector_type(2)));
typedef unsigned u32x4  __attribute__((ext_vector_type(4)));
typedef __attribute__((address_space(3))) s16x4* lds_s16x4_p;
typedef __attribute__((address_space(3))) char* lds_char_p;

#define DI __device__ __forceinline__
#define W_NT false
#define X_NT false
#define O_NT 0
#define ROT 1
#define QPW 2, 2, 0, 0, 0, 2, 2, 2, 1, 1, 0, 0, 0, 0, 0, 0
#define QPX 1, 1, 0, 0, 0, 1, 1, 1, 0, 1, 0, 0, 0, 0, 0, 0
#define QPE 0, 0, 4, 0, 0, 0, 0, 0, 0, 0, 4, 2, 2, 2, 1, 1
#define FIRST_NT false
#define ROTK 1
#define USE_PRIO 1
#define PRIO(n) __builtin_amdgcn_s_setprio(n)
#define WO_POL ""
#define OL_POL ""
#define SPREAD 1
#define TAIL 1
#define ACQ 0
#define ACQUIRE()
#define GRID1 256

constexpr int NB = 8, SQ = 144, DM = 512, NH = 32, SK = 512, DH = 128, HD = NH * DH;
constexpr int NQT = SQ / 16;
constexpr int TILE_B = 64 * 256;
constexpr int SLOT_B = 2 * TILE_B;
constexpr int LDS1 = 5 * SLOT_B;
constexpr int OH_B = NQT * 4096, OW_B = 2 * OH_B;
constexpr size_t PART_OFF = (size_t)256 * OW_B, FLAG_OFF = 2 * PART_OFF, FLAG2_OFF = FLAG_OFF + 256 * 128, GEN_OFF = FLAG2_OFF + 256 * 8 * 128;
constexpr int SPIN_MAX = 1 << 18;
constexpr float QSCALE = 0.08838834764831845f * 1.4426950408889634f;
constexpr float RESCALE_THR = 8.0f;

template <class F, int... Is> DI void static_for_impl(F&& f, std::integer_sequence<int, Is...>) { (f(std::integral_constant<int, Is>{}), ...); }
template <int N, class F> DI void static_for(F&& f) { static_for_impl(f, std::make_integer_sequence<int, N>{}); }
#define FOR_I(N) static_for<N>([&](auto ic_) __attribute__((always_inline)) { constexpr int i = decltype(ic_)::value;
#define END_FOR });

DI f32x4 mfma32(bf16x8 a, bf16x8 b, f32x4 c) { f32x4 d = __builtin_amdgcn_mfma_f32_16x16x32_bf16(a, b, c, 0, 0, 0); asm volatile("" :: "v"(d)); return d; }
DI f32x16 mfmaL(bf16x8 a, bf16x8 b, f32x16 c) { f32x16 d = __builtin_amdgcn_mfma_f32_32x32x16_bf16(a, b, c, 0, 0, 0); asm volatile("" :: "v"(d)); return d; }
DI f32x4 mfma16(s16x4 a, s16x4 b, f32x4 c) { f32x4 d = __builtin_amdgcn_mfma_f32_16x16x16bf16_1k(a, b, c, 0, 0, 0); asm volatile("" :: "v"(d)); return d; }
DI unsigned pk2(float lo, float hi) { f32x2 v = {lo, hi}; return __builtin_bit_cast(unsigned, __builtin_convertvector(v, bf16x2)); }
DI s16x4 tr_read(lds_char_p p) { return __builtin_amdgcn_ds_read_tr16_b64_v4i16((lds_s16x4_p)p); }
DI bf16x8 tr_frag(lds_char_p p, int second) {
  s16x4 lo = tr_read(p), hi = tr_read(p + second);
  return __builtin_bit_cast(bf16x8, __builtin_shufflevector(lo, hi, 0, 1, 2, 3, 4, 5, 6, 7));
}
DI bf16x8 pack8(f32x4 a, f32x4 b) {
  u32x4 w = {pk2(a[0], a[1]), pk2(a[2], a[3]), pk2(b[0], b[1]), pk2(b[2], b[3])};
  return __builtin_bit_cast(bf16x8, w);
}
DI float max3(float a, float b, float c) { return __builtin_fmaxf(__builtin_fmaxf(a, b), c); }
DI float xh_max(float x) {
  auto a = __builtin_amdgcn_permlane32_swap(__float_as_uint(x), __float_as_uint(x), false, false);
  return fmaxf(__uint_as_float(a[0]), __uint_as_float(a[1]));
}
DI float xh_sum(float x) {
  auto a = __builtin_amdgcn_permlane32_swap(__float_as_uint(x), __float_as_uint(x), false, false);
  return __uint_as_float(a[0]) + __uint_as_float(a[1]);
}
template <int B> DI bf16x8 pack8s(const f32x16& v, float k) {
  u32x4 w = {pk2(v[B] * k, v[B + 1] * k), pk2(v[B + 2] * k, v[B + 3] * k), pk2(v[B + 4] * k, v[B + 5] * k), pk2(v[B + 6] * k, v[B + 7] * k)};
  return __builtin_bit_cast(bf16x8, w);
}
DI float max16(float m, const f32x16& v) {
#pragma unroll
  for (int e = 0; e < 16; e += 2) m = max3(m, v[e], v[e + 1]);
  return m;
}
DI float xg_max(float x) {
  auto a = __builtin_amdgcn_permlane32_swap(__float_as_uint(x), __float_as_uint(x), false, false);
  x = fmaxf(__uint_as_float(a[0]), __uint_as_float(a[1]));
  auto b = __builtin_amdgcn_permlane16_swap(__float_as_uint(x), __float_as_uint(x), false, false);
  return fmaxf(__uint_as_float(b[0]), __uint_as_float(b[1]));
}
DI float xg_sum(float x) {
  auto a = __builtin_amdgcn_permlane32_swap(__float_as_uint(x), __float_as_uint(x), false, false);
  x = __uint_as_float(a[0]) + __uint_as_float(a[1]);
  auto b = __builtin_amdgcn_permlane16_swap(__float_as_uint(x), __float_as_uint(x), false, false);
  return __uint_as_float(b[0]) + __uint_as_float(b[1]);
}

template <int OFF, bool NT> DI void dma1(unsigned lds, const char* base, unsigned v) {
  lds = __builtin_amdgcn_readfirstlane(lds);
  if constexpr (NT) asm volatile("s_mov_b32 m0, %0\n\ts_nop 0\n\tglobal_load_lds_dwordx4 %2, %1 offset:%3 nt" :: "s"(lds), "s"(base), "v"(v), "i"(OFF) : "memory");
  else              asm volatile("s_mov_b32 m0, %0\n\ts_nop 0\n\tglobal_load_lds_dwordx4 %2, %1 offset:%3" :: "s"(lds), "s"(base), "v"(v), "i"(OFF) : "memory");
}
template <int OFF> DI void gload16nt(bf16x8& dst, const char* base, unsigned voff) {
  asm volatile("global_load_dwordx4 %0, %1, %2 offset:%3 nt" : "=v"(dst) : "v"(voff), "s"(base), "i"(OFF) : "memory");
}
DI void pin(bf16x8& x) { asm volatile("" : "+v"(x)); }
DI void keep(unsigned x) { asm volatile("" :: "v"(x)); }
DI void opaque(unsigned& x) { asm volatile("" : "+v"(x)); }
DI void dma_quarter(unsigned lds, const char* base, unsigned v) {
  lds = __builtin_amdgcn_readfirstlane(lds);
  asm volatile("s_mov_b32 m0, %0\n\ts_mov_b64 exec, 0xffff\n\tglobal_load_lds_dwordx4 %2, %1\n\ts_mov_b64 exec, -1" :: "s"(lds), "s"(base), "v"(v) : "memory");
}
DI void gload16_dev(bf16x8& dst, const char* base, unsigned voff) {
  asm volatile("global_load_dwordx4 %0, %1, %2 sc1" : "=v"(dst) : "v"(voff), "s"(base) : "memory");
}
DI void gload8_dev(u32x2& dst, const char* base, unsigned voff) {
  asm volatile("global_load_dwordx2 %0, %1, %2 sc1" : "=v"(dst) : "v"(voff), "s"(base) : "memory");
}
DI void dma_dev(unsigned lds, const char* base, unsigned v) {
  lds = __builtin_amdgcn_readfirstlane(lds);
  asm volatile("s_mov_b32 m0, %0\n\ts_nop 0\n\tglobal_load_lds_dwordx4 %2, %1 sc1" :: "s"(lds), "s"(base), "v"(v) : "memory");
}
DI void poll_tag(const char* base, unsigned voff, unsigned lo, unsigned hi) {
#pragma unroll 1
  for (int spin = 0; spin < SPIN_MAX; ++spin) {
    u32x2 f;
    asm volatile("global_load_dwordx2 %0, %1, %2 sc1\n\ts_waitcnt vmcnt(0)" : "=&v"(f) : "v"(voff), "s"(base) : "memory");
    if (__all(f[0] == lo && f[1] == hi)) break;
    __builtin_amdgcn_s_sleep(2);
  }
}
DI void gstore16_dev(char* p, bf16x8 v) { asm volatile("global_store_dwordx4 %0, %1, off sc1\n\ts_nop 1" :: "v"(p), "v"(v) : "memory"); }
DI void gstore8_dev(char* p, u32x2 v) { asm volatile("global_store_dwordx2 %0, %1, off sc1" :: "v"(p), "v"(v) : "memory"); }
#define WAIT_VM(n) asm volatile("s_waitcnt vmcnt(" #n ")" ::: "memory")
#define MEM_FENCE() asm volatile("" ::: "memory")
#define WAIT_LGKM(n) do { MEM_FENCE(); __builtin_amdgcn_s_waitcnt(0xC07F | ((n) << 8)); MEM_FENCE(); } while (0)
#define BARRIER() do { WAIT_LGKM(0); __builtin_amdgcn_s_barrier(); MEM_FENCE(); } while (0)
#define SCHED_FENCE() do { MEM_FENCE(); __builtin_amdgcn_sched_barrier(0); } while (0)

#define DIAG_BASE (64u << 20)
#define STAMP(i) do { SCHED_FENCE(); unsigned long long t_, c_; \
    asm volatile("s_memrealtime %0\n\ts_memtime %1\n\ts_waitcnt lgkmcnt(0)" : "=s"(t_), "=s"(c_) :: "memory"); \
    dg_t[i] = (unsigned)t_; dg_c[i] = (unsigned)c_; SCHED_FENCE(); } while (0)
#define DIAG_DECL(n) unsigned dg_t[n], dg_c[n], dg_last = 0, dg_acc[8] = {0, 0, 0, 0, 0, 0, 0, 0}
#define STEPSTAMP(t) do { SCHED_FENCE(); unsigned long long t_; asm volatile("s_memrealtime %0\n\ts_waitcnt lgkmcnt(0)" : "=s"(t_) :: "memory"); \
    _Pragma("unroll") for (int k_ = 0; k_ < 8; ++k_) dg_acc[k_] = (t) == k_ ? (unsigned)t_ : dg_acc[k_]; SCHED_FENCE(); } while (0)
#define DIAG_PARAMS , unsigned& dg_last, unsigned* dg_acc
#define DIAG_ARGS , dg_last, dg_acc
#define TICK(i)
#define DIAG_FLUSH(n, ptr, slot) do { if ((threadIdx.x & 63) == 0) { unsigned* d_ = (unsigned*)((char*)(ptr) + DIAG_BASE) + (size_t)(slot) * 64; \
    for (int i_ = 0; i_ < n; ++i_) { d_[2 * i_] = dg_t[i_]; d_[2 * i_ + 1] = dg_c[i_]; } \
    for (int i_ = 0; i_ < 8; ++i_) d_[40 + i_] = dg_acc[i_]; \
    unsigned hw_, xc_; asm volatile("s_getreg_b32 %0, hwreg(HW_REG_HW_ID)\n\ts_getreg_b32 %1, hwreg(HW_REG_XCC_ID)" : "=s"(hw_), "=s"(xc_)); d_[62] = hw_; d_[63] = xc_; } } while (0)

DI bf16x8 q_frag(f32x4 t0, f32x4 t1) {
  unsigned a0 = pk2(t0[0] * QSCALE, t0[1] * QSCALE), a1 = pk2(t0[2] * QSCALE, t0[3] * QSCALE);
  unsigned b0 = pk2(t1[0] * QSCALE, t1[1] * QSCALE), b1 = pk2(t1[2] * QSCALE, t1[3] * QSCALE);
  auto p0 = __builtin_amdgcn_permlane32_swap(a0, b0, false, false);
  auto p1 = __builtin_amdgcn_permlane32_swap(a1, b1, false, false);
  auto r0 = __builtin_amdgcn_permlane16_swap(p0[0], p0[1], false, false);
  auto r1 = __builtin_amdgcn_permlane16_swap(p1[0], p1[1], false, false);
  u32x4 w = {r0[0], r1[0], r0[1], r1[1]};
  return __builtin_bit_cast(bf16x8, w);
}

DI lds_char_p lds_at(unsigned a) { return (lds_char_p)(uintptr_t)a; }
#define LDS_RD(T, p) (*(__attribute__((address_space(3))) T*)(p))
template <int N> DI void wait_vm() { asm volatile("s_waitcnt vmcnt(%0)" :: "i"(N) : "memory"); }

struct AttnState {
  bf16x8 qf[8], q9[4];
  f32x16 o[4];
  f32x4 o9[8];
  f32x16 c;
  float c9;
  float ls, ls9;
  f32x16 sE, sO;
  f32x4 s9;
  u32x4 pE[2];
  bf16x8 kc[4];
  unsigned rdk, rdk9, rdv, rdv9, vk0, ldsw;
  const char *kg, *vg;
  const char* wk;
  const char* wt;
  unsigned ldso;
  int hp;
  int rk;
};
#define EXP2X(v, e, dst, l) do { const float x0_ = __builtin_amdgcn_exp2f(v[e]); float x1_ = __builtin_amdgcn_exp2f(v[(e) + 1]); \
    dst = pk2(x0_, x1_); l += x0_; asm volatile("" : "+v"(l), "+v"(dst), "+v"(x1_)); v[(e) + 1] = x1_; } while (0)
#define ADD1(l, x) do { l += (x); asm volatile("" : "+v"(l)); } while (0)
#define MAX2X(m, v, e) do { m = max3(m, v[e], v[(e) + 1]); asm volatile("" : "+v"(m)); } while (0)
#define BF8(x) __builtin_bit_cast(bf16x8, x)
#define K_RD(kb, s) LDS_RD(bf16x8, (((s) & 1) ? ko : ke) + 8192 * (kb) + 256 * ((s) >> 1))
#define V_RD(kb, f) tr_frag(vr + 8192 * (kb) + 4096 * ((f) >> 2) + 256 * ((f) & 3), 2048)
template <int I> DI void dma_k(const AttnState& A, int slot, int t) {
  dma1<1024 * I, true>(A.ldsw + slot * SLOT_B, A.kg + ((t + A.rk) & 7) * TILE_B, A.vk0 ^ (16 * I));
}
template <int I> DI void dma_v(const AttnState& A, int slot, int t) {
  dma1<1024 * I, true>(A.ldsw + slot * SLOT_B, A.vg + ((t + A.rk) & 7) * TILE_B, A.vk0 ^ (32 * (I & 1)));
}
DI void dma_k4(const AttnState& A, int slot, int t) { dma_k<0>(A, slot, t); dma_k<1>(A, slot, t); dma_k<2>(A, slot, t); dma_k<3>(A, slot, t); }
DI void dma_v4(const AttnState& A, int slot, int t) { dma_v<0>(A, slot, t); dma_v<1>(A, slot, t); dma_v<2>(A, slot, t); dma_v<3>(A, slot, t); }
template <int I> DI void dma_kv(const AttnState& A, int slotk, int slotv, int t) {
  if constexpr (I < 4) dma_k<I>(A, slotk, t); else dma_v<I - 4>(A, slotv, t);
}
DI unsigned lane_id() {
  unsigned ln;
  asm volatile("v_mbcnt_lo_u32_b32 %0, -1, 0\n\tv_mbcnt_hi_u32_b32 %0, -1, %0" : "=v"(ln));
  return ln;
}
template <int I> DI void dma_wo(const AttnState& A, int peer, unsigned v) {
  dma1<0, false>(A.ldso + 1024 * I, A.wk + (size_t)(256 * peer + 16 * I) * DM * 2, v);
}
DI void rescale_own(AttnState& A, f32x16& s) {
  const float d = xh_max(max16(0.f, s)), a = __builtin_amdgcn_exp2f(-d);
  A.c -= d; s -= d; A.ls *= a;
#pragma unroll
  for (int i = 0; i < 4; ++i) A.o[i] *= a;
}
DI void rescale_shared(AttnState& A) {
  const float d = xg_max(max3(max3(0.f, A.s9[0], A.s9[1]), A.s9[2], A.s9[3])), a = __builtin_amdgcn_exp2f(-d);
  A.c9 -= d; A.s9 -= d; A.ls9 *= a;
#pragma unroll
  for (int i = 0; i < 8; ++i) A.o9[i] *= a;
}
DI void attn_x2a(AttnState& A, lds_char_p ke, lds_char_p ko, bf16x8* k2) {
  FOR_I(4)
    A.sO = mfmaL(k2[i], A.qf[i], i ? A.sO : A.c);
    A.kc[i] = K_RD(1, 4 + i);
    EXP2X(A.sE, 2 * i, A.pE[0][i], A.ls);
    SCHED_FENCE();
  END_FOR
}
template <int MODE> DI void attn_step(AttnState& A, int t, int sk DIAG_PARAMS) {
  constexpr bool LAST = MODE == 2;
  const int sv = sk == 4 ? 0 : sk + 1, sn = sv == 4 ? 0 : sv + 1, sp = sk == 0 ? 4 : sk - 1;
  TICK(0);
  wait_vm<(LAST ? 1 : MODE == 1 ? 0 : 4)>();
  TICK(1);
  BARRIER();
  TICK(2);
  STEPSTAMP(t);
  PRIO(3);
  lds_char_p vr = lds_at(A.rdv + sv * SLOT_B);
  bf16x8 va[8], vb[8], k0[8], k2[4], k9[4];
  s16x4 v9[8];
  u32x4 pO[2];
  u32x2 p9;
  float mx;
  bool grow;
  FOR_I(4)
    A.sO = mfmaL(A.kc[i], A.qf[4 + i], A.sO);
    if constexpr (i < 2) { va[2 * i] = V_RD(0, 2 * i); va[2 * i + 1] = V_RD(0, 2 * i + 1); }
    else va[2 + i] = V_RD(0, 2 + i);
    EXP2X(A.sE, 8 + 2 * i, A.pE[1][i], A.ls);
    SCHED_FENCE();
  END_FOR
  lds_char_p ve9 = lds_at(A.rdv9 + sv * SLOT_B), vo9 = lds_at((A.rdv9 + sv * SLOT_B) ^ 32);
  lds_char_p ke = lds_at(A.rdk + sn * SLOT_B), ko = lds_at((A.rdk + sn * SLOT_B) ^ 32);
  FOR_I(8)
    A.o[i & 3] = mfmaL(va[i], BF8(A.pE[i >> 2]), A.o[i & 3]);
    if constexpr (i < 2) va[6 + i] = V_RD(0, 6 + i);
    else if constexpr (i < 6) { v9[2 * (i - 2)] = tr_read(ve9 + 256 * (i - 2)); v9[2 * (i - 2) + 1] = tr_read(vo9 + 256 * (i - 2)); }
    else if constexpr (!LAST) k0[i - 6] = K_RD(0, i - 6);
    if constexpr (MODE == 0 && !SPREAD) dma_kv<i>(A, sp, sk, t + 2);
    if constexpr ((MODE == 0 || MODE == 3) && SPREAD && (i & 1) == 0) dma_k<(i >> 1)>(A, sp, t + 2);
    if constexpr (LAST) dma_wo<i>(A, A.hp, A.vk0);
    if constexpr (TAIL && MODE == 1 && (i & 1) == 0) dma_v<(i >> 1)>(A, sk < 2 ? sk + 3 : sk - 2, t + 1);
    if constexpr (MODE == 1 && i == 7) {
      const unsigned l = __builtin_amdgcn_readfirstlane(A.ldso - 2 * SLOT_B);
      asm volatile("s_mov_b32 m0, %0\n\ts_nop 0\n\tglobal_load_lds_dword %2, %1" :: "s"(l), "s"(A.wt), "v"(lane_id() * 64) : "memory");
    }
    ADD1(A.ls, A.sE[2 * i + 1]);
    if constexpr (i == 7) PRIO(2);
    if constexpr (i == 0) { mx = 0.f; EXP2X(A.s9, 0, p9[0], A.ls9); }
    if constexpr (i == 1) EXP2X(A.s9, 2, p9[1], A.ls9);
    if constexpr (i >= 2) MAX2X(mx, A.sO, 2 * (i - 2));
    SCHED_FENCE();
  END_FOR
  FOR_I(4)
    A.o9[2 * i] = mfma16(v9[2 * i], __builtin_bit_cast(s16x4, p9), A.o9[2 * i]);
    if constexpr (LAST) { vb[2 * i] = V_RD(1, 2 * i); vb[2 * i + 1] = V_RD(1, 2 * i + 1); }
    else k0[2 + i] = K_RD(0, 2 + i);
    A.o9[2 * i + 1] = mfma16(v9[2 * i + 1], __builtin_bit_cast(s16x4, p9), A.o9[2 * i + 1]);
    if constexpr (i < 2) { MAX2X(mx, A.sO, 12 + 2 * i); ADD1(A.ls9, A.s9[2 * i + 1]); }
    if constexpr (i == 2) grow = __any(mx > RESCALE_THR);
    SCHED_FENCE();
  END_FOR
  if (__builtin_expect(grow, 0)) rescale_own(A, A.sO);
  SCHED_FENCE();
  lds_char_p kn9 = lds_at(A.rdk9 + sn * SLOT_B);
  FOR_I(8)
    if constexpr (!LAST) {
      A.sE = mfmaL(k0[i], A.qf[i], i ? A.sE : A.c);
      if constexpr (i < 2) k0[6 + i] = K_RD(0, 6 + i);
      else if constexpr (i < 6) k9[i - 2] = LDS_RD(bf16x8, kn9 + 256 * (i - 2));
      else vb[i - 6] = V_RD(1, i - 6);
    }
    EXP2X(A.sO, 2 * i, pO[i >> 2][i & 3], A.ls);
    if constexpr (i == 6) PRIO(1);
    if constexpr (MODE == 0 && SPREAD && (i == 0 || i == 5)) dma_v<(i ? 1 : 0)>(A, sk, t + 2);
    SCHED_FENCE();
  END_FOR
  if constexpr (!LAST) {
    FOR_I(2)
      A.s9 = mfma32(k9[2 * i], A.q9[2 * i], i ? A.s9 : f32x4{A.c9, A.c9, A.c9, A.c9});
      vb[2 + 2 * i] = V_RD(1, 2 + 2 * i);
      A.s9 = mfma32(k9[2 * i + 1], A.q9[2 * i + 1], A.s9);
      vb[3 + 2 * i] = V_RD(1, 3 + 2 * i);
      SCHED_FENCE();
    END_FOR
  }
  FOR_I(8)
    A.o[i & 3] = mfmaL(vb[i], BF8(pO[i >> 2]), A.o[i & 3]);
    if constexpr (!LAST) {
      if constexpr (i < 2) vb[6 + i] = V_RD(1, 6 + i);
      else if constexpr (i < 6) k2[i - 2] = K_RD(1, i - 2);
    }
    ADD1(A.ls, A.sO[2 * i + 1]);
    if constexpr (i == 6) PRIO(0);
    if constexpr (MODE == 0 && SPREAD && (i == 0 || i == 5)) dma_v<(i ? 3 : 2)>(A, sk, t + 2);
    if constexpr (!LAST) {
      if constexpr (i == 0) mx = 0.f;
      if constexpr (i < 4) { MAX2X(mx, A.sE, 4 * i); MAX2X(mx, A.sE, 4 * i + 2); }
      if constexpr (i == 4 || i == 5) MAX2X(mx, A.s9, 2 * (i - 4));
      if constexpr (i == 6) grow = __any(mx > RESCALE_THR);
    }
    SCHED_FENCE();
  END_FOR
  if constexpr (!LAST) {
    if (__builtin_expect(grow, 0)) { rescale_own(A, A.sE); rescale_shared(A); }
    SCHED_FENCE();
    attn_x2a(A, ke, ko, k2);
  }
}

constexpr int QP_W[16] = {QPW}, QP_X[16] = {QPX}, QP_E[16] = {QPE};
constexpr int qp_sum(const int* t, int s) { int n = 0; for (int j = 0; j < s; ++j) n += t[j]; return n; }
constexpr int qp_w0(int s) { return 4 + qp_sum(QP_W, s); }
constexpr int qp_x0(int s) { return 2 + qp_sum(QP_X, s); }
constexpr int qp_e0(int s) { return qp_sum(QP_E, s); }
constexpr int qp_n(int s) { return 2 * QP_W[s] + 3 * QP_X[s] + QP_E[s]; }
constexpr int qp_cum(int s) { int n = 14; for (int j = 0; j < s; ++j) n += qp_n(j); return n; }
constexpr int qp_endw(int i) {
  if (i <= 3) return 7 * (i >> 1) + 5 + 2 * (i & 1);
  int s = 0; while (qp_w0(s + 1) <= i) ++s;
  return qp_cum(s) + 2 * (i - qp_w0(s) + 1);
}
constexpr int qp_endx(int c) {
  if (c <= 1) return 7 * c + 3;
  int s = 0; while (qp_x0(s + 1) <= c) ++s;
  return qp_cum(s) + 2 * QP_W[s] + 3 * (c - qp_x0(s) + 1);
}
constexpr int qp_ende(int e) { int s = 0; while (qp_e0(s + 1) <= e) ++s; return qp_cum(s) + 2 * QP_W[s] + 3 * QP_X[s] + e - qp_e0(s) + 1; }
constexpr int qp_max(int a, int b) { return a > b ? a : b; }
constexpr int qp_wait(int s) {
  const int need = s <= 14 ? qp_max(qp_endw(s + 1), qp_endx((s + 1) >> 1)) : 0;
  return qp_cum(s) - qp_max(need, s == 14 ? qp_ende(3) : s == 15 ? qp_ende(7) : 0);
}
constexpr bool qp_ok() {
  if (qp_w0(16) != 16 || qp_x0(16) != 8 || qp_e0(16) != 16 || qp_e0(13) > 12) return false;
  for (int s = 0; s < 16; ++s) {
    if (qp_wait(s) < 0 || qp_wait(s) > 63) return false;
    if (qp_w0(s + 1) - 1 > s + 6) return false;
    if (qp_x0(s + 1) - 1 >= 3 && 2 * (qp_x0(s + 1) - 4) + 1 > s) return false;
  }
  return true;
}
static_assert(qp_ok(), "projection schedule");

__global__ __launch_bounds__(512, 2)
void fused_kernel(const char* __restrict__ xA, const char* __restrict__ kA, const char* __restrict__ vA, const char* __restrict__ wqA,
                  const char* __restrict__ woA, const char* __restrict__ xB, const char* __restrict__ kB, const char* __restrict__ vB,
                  const char* __restrict__ wqB, const char* __restrict__ woB, char* ows, char* __restrict__ out) {
  extern __shared__ __attribute__((aligned(1024))) char smem_raw[];
  const int tid = threadIdx.x, lane = tid & 63, r = lane & 15, g = lane >> 4, r32 = lane & 31, hh = lane >> 5;
  const int wv = __builtin_amdgcn_readfirstlane(tid >> 6), w = wv & 3, gq = wv >> 2;
  const unsigned ldsb = (unsigned)(uintptr_t)(lds_char_p)smem_raw, lds0 = ldsb + gq * TILE_B;
  const int bid = blockIdx.x, xcd = bid & 7, idx = bid >> 3;
  const int br = xcd >> 2, h = 8 * (xcd & 3) + 2 * (idx & 3) + gq, b = idx >> 2;
  const int rot = ROT ? (b + 2 * (idx & 3)) & 7 : 0;
  const char* xg = (br ? xB : xA) + (size_t)b * SQ * DM * 2;
  const char* wg = (br ? wqB : wqA) + h * DH * 2;
  const int q4 = (lane >> 2) & 3, p4 = lane & 3, r8 = (lane >> 2) & 7, blk = g & 1;

  AttnState A;
  A.kg = (br ? kB : kA) + (size_t)(b * NH + h) * SK * DH * 2;
  A.vg = (br ? vB : vA) + (size_t)(b * NH + h) * SK * DH * 2;
  A.rk = ROTK ? (h + b) & 7 : 0;
  A.ldsw = lds0 + w * 4096;
  A.vk0 = (16 * w + q4) * 256 + 64 * g + 16 * p4;
  DIAG_DECL(10);
  STAMP(0);
  unsigned tag_lo, tag_hi;
  u32x2 gen;
  gload8_dev(gen, ows + GEN_OFF, 0);
  {
    constexpr int XOFF = 3 * SLOT_B, XCH = SQ * 128, EXOFF = XOFF + 3 * XCH;
    const unsigned vw0 = (8 * w + r8) * (HD * 2) + 64 * hh + 16 * (p4 ^ (2 * (w & 1)));
#define WPIECE(s) (((((s) + 2) % 6) >> 1) * SLOT_B + ((s) & 1) * 8192)
#define DMA_W(s, i) dma1<128 * (i), (FIRST_NT && (s) < 2)>(lds0 + WPIECE(s) + w * 2048 + 896 * (i), wg + (size_t)(((s) + 2 * rot) & 15) * 32 * HD * 2, vw0)
    unsigned vx[3];
    FOR_I(3) const int row = 18 * wv + 8 * i + (i < 2 ? lane >> 3 : (lane >> 3) & 1);
             vx[i] = row * (DM * 2) + 16 * ((lane & 7) ^ ((row >> 1) & 7)); END_FOR
#define XPOS(c) ((((c) + 1) % 3) * XCH)
#define DMA_X(c, i) do { const unsigned l_ = ldsb + XOFF + XPOS(c) + wv * (18 * 128) + 1024 * (i); const char* s_ = xg + 128 * (((c) + rot) & 7); \
      if constexpr ((i) < 2) dma1<0, (FIRST_NT && (c) < 2)>(l_, s_, vx[i]); else dma_quarter(l_, s_, vx[2]); } while (0)
    unsigned xr[6];
    FOR_I(4) xr[i] = (ldsb + XOFF + (32 * w + r32) * 128 + 16 * (hh ^ ((r32 >> 1) & 7))) ^ (32 * i); END_FOR
    FOR_I(2) xr[4 + i] = (ldsb + XOFF + (128 + r) * 128 + 16 * (g ^ (r >> 1))) ^ (64 * i); END_FOR
#define X_RD(set, s, i) xf[set][i] = LDS_RD(bf16x8, lds_at(xr[(i) < 2 ? 2 * ((s) & 1) + (i) : 4 + ((s) & 1)]) + XPOS((s) >> 1))
    unsigned rw[6];
    rw[0] = lds0 + 2048 * hh + 64 * q4 + 32 * (blk ^ hh) + 8 * (((p4 & 1) << 1) | (p4 >> 1));
    rw[1] = lds0 + 2048 * g + 64 * q4 + 32 * blk + 8 * p4 + 512 * w; rw[2] = rw[1] ^ 32;
    FOR_I(3) rw[3 + i] = rw[i] + 2 * SLOT_B; END_FOR
    FOR_I(6) opaque(rw[i]); opaque(xr[i]); END_FOR
#define W_RD(set, s, i) wf[set][i] = tr_frag(lds_at(rw[((i) < 8 ? 0 : 1 + ((i) & 1)) + (WPIECE(s) >= 2 * SLOT_B ? 3 : 0)]) + \
                                             (((i) < 8 ? 4096 * (((i) >> 2) & 1) + 512 * ((i) & 3) : 0) + WPIECE(s) % (2 * SLOT_B)), 256)
    bf16x8 er[12];
    auto early = [&](auto ec_) __attribute__((always_inline)) {
      constexpr int e = decltype(ec_)::value, j = e & 3;
      if constexpr (e >= 4 && e < 8) gload16nt<1024 * j>(er[e], A.vg + A.rk * TILE_B, A.vk0 ^ (32 * (j & 1)));
      else if constexpr (e < 12) gload16nt<1024 * j>(er[e], A.kg + (((e >> 3) + A.rk) & 7) * TILE_B, A.vk0 ^ (16 * j));
      else dma_v<j>(A, 3, 1);
    };
    lds_char_p dump = lds_at(A.ldsw + lane * 16);
    bf16x8 xf[2][3], wf[2][10];
    f32x16 acc[4];
    f32x4 acc9[2];
#pragma unroll
    for (int i = 0; i < 4; ++i) acc[i] = f32x16{};
    acc9[0] = f32x4{0, 0, 0, 0}; acc9[1] = f32x4{0, 0, 0, 0};

    FOR_I(2)
      DMA_X(i, 0); DMA_X(i, 1); DMA_X(i, 2);
      DMA_W(2 * i, 0); DMA_W(2 * i, 1); DMA_W(2 * i + 1, 0); DMA_W(2 * i + 1, 1);
    END_FOR
    wait_vm<9>();
    asm volatile("" : "+v"(gen));
    tag_lo = __builtin_amdgcn_readfirstlane(gen[0]) + 1u;
    tag_hi = __builtin_amdgcn_readfirstlane(gen[1]);
    asm volatile("" : "+s"(tag_lo), "+s"(tag_hi));
    BARRIER();
    STAMP(1);
    FOR_I(3) X_RD(0, 0, i); END_FOR
    FOR_I(10) W_RD(0, 0, i); END_FOR
    static_for<16>([&](auto sc_) __attribute__((always_inline)) {
      constexpr int s = decltype(sc_)::value;
      constexpr int cs = s & 1, ns = cs ^ 1;
      wait_vm<qp_wait(s)>();
      BARRIER();
      if constexpr (s == 8) STAMP(2);
      SCHED_FENCE();
      FOR_I(9)
        if constexpr (i == 0) PRIO(3);
        if constexpr (i == 3) PRIO(2);
        if constexpr (i == 5) PRIO(1);
        if constexpr (i == 7) PRIO(0);
        if constexpr (i < 8) acc[i & 3] = mfmaL(wf[cs][i], xf[cs][i >> 2], acc[i & 3]);
        else { acc9[0] = mfma32(wf[cs][8], xf[cs][2], acc9[0]); acc9[1] = mfma32(wf[cs][9], xf[cs][2], acc9[1]); }
        SCHED_FENCE();
        if constexpr (s < 15) {
          if constexpr (i < 7) W_RD(ns, s + 1, i);
          if constexpr (i < 6) { if constexpr (i & 1) W_RD(ns, s + 1, 7 + (i >> 1)); else X_RD(ns, s + 1, i >> 1); }
        }
        static_for<qp_n(s)>([&](auto kc_) __attribute__((always_inline)) {
          constexpr int k = decltype(kc_)::value, kx = k - 2 * QP_W[s], ke = kx - 3 * QP_X[s];
          if constexpr (k * 9 / qp_n(s) == i) {
            if constexpr (kx < 0) DMA_W(qp_w0(s) + (k >> 1), k & 1);
            else if constexpr (ke < 0) DMA_X(qp_x0(s) + kx / 3, kx % 3);
            else early(std::integral_constant<int, qp_e0(s) + ke>{});
          }
        });
        if constexpr (s >= 14 && i < 4) { pin(er[4 * (s - 14) + i]); LDS_RD(bf16x8, dump + (s - 14) * SLOT_B + 1024 * i) = er[4 * (s - 14) + i]; }
        SCHED_FENCE();
      END_FOR
    });
    lds_char_p ex = lds_at(ldsb + EXOFF + gq * 4096 + lane * 16);
    LDS_RD(bf16x8, ex + w * 1024) = q_frag(acc9[0], acc9[1]);
    FOR_I(8) A.qf[i] = pack8s<8 * (i & 1)>(acc[i >> 1], QSCALE); END_FOR
    wait_vm<qp_cum(16) - qp_ende(11)>();
    FOR_I(4) pin(er[8 + i]); LDS_RD(bf16x8, dump + 2 * SLOT_B + 1024 * i) = er[8 + i]; END_FOR
    BARRIER();
    FOR_I(4) A.q9[i] = LDS_RD(bf16x8, ex + i * 1024); END_FOR
#undef WPIECE
#undef DMA_W
#undef XPOS
#undef DMA_X
#undef X_RD
#undef W_RD
  }

  STAMP(3);
#pragma unroll
  for (int i = 0; i < 4; ++i) A.o[i] = f32x16{};
#pragma unroll
  for (int i = 0; i < 8; ++i) A.o9[i] = f32x4{0, 0, 0, 0};
  A.rdk = lds0 + 1024 * (r32 >> 2) + 64 * (r32 & 3) + 16 * (hh ^ ((r32 >> 2) & 3));
  A.rdk9 = lds0 + 4096 * w + 1024 * (r >> 2) + 64 * (r & 3) + 16 * (g ^ (r >> 2));
  A.rdv = lds0 + 1024 * hh + 64 * q4 + 32 * (blk ^ hh) + 8 * p4;
  A.rdv9 = lds0 + 4096 * w + 1024 * g + 64 * q4 + 32 * blk + 8 * p4;
  {
    lds_char_p ke = lds_at(A.rdk), ko = lds_at(A.rdk ^ 32);
    bf16x8 k0[8], k2[4], k9[4];
    A.ls = 0.f; A.ls9 = 0.f;
    FOR_I(8) k0[i] = K_RD(0, i); END_FOR
    FOR_I(4) k9[i] = LDS_RD(bf16x8, lds_at(A.rdk9) + 256 * i); k2[i] = K_RD(1, i); END_FOR
    A.sE = f32x16{}; A.s9 = f32x4{0, 0, 0, 0};
    FOR_I(8) A.sE = mfmaL(k0[i], A.qf[i], A.sE); END_FOR
    FOR_I(4) A.s9 = mfma32(k9[i], A.q9[i], A.s9); END_FOR
    const float m = xh_max(max16(A.sE[0], A.sE));
    A.c = f32x16{} - m; A.sE -= m;
    const float m9 = xg_max(fmaxf(max3(A.s9[0], A.s9[1], A.s9[2]), A.s9[3]));
    A.c9 = -m9; A.s9 -= m9;
    SCHED_FENCE();
    attn_x2a(A, ke, ko, k2);
  }
  TICK(7);
  int sk = 0;
#pragma unroll 1
  for (int t = 0; t < (TAIL ? 5 : 6); ++t) {
    asm volatile("" : "+v"(A.vk0), "+s"(sk));
    attn_step<0>(A, t, sk DIAG_ARGS);
    sk = sk >= 3 ? sk - 3 : sk + 2;
  }
  const int hg = xcd & 3;
  A.hp = idx & 3;
  A.wt = (br ? woB : woA) + (size_t)hg * 1024 * DM * 2 + (idx * 8 + wv) * 4096;
  A.wk = (br ? woB : woA) + (size_t)(1024 * hg + 128 * gq) * DM * 2 + A.hp * 256 + w * 64;
  A.ldso = ldsb + 3 * SLOT_B + wv * 8192;
  attn_step<3>(A, 5, sk DIAG_ARGS); sk += 2;
  attn_step<1>(A, 6, sk DIAG_ARGS);
  { const unsigned l = lane_id(); A.vk0 = (l >> 2) * (DM * 2) + 16 * ((l & 3) ^ ((l >> 3) & 2)); }
  attn_step<2>(A, 7, sk + 2 DIAG_ARGS);
  TICK(0);

  STAMP(4);
  const int ln = lane_id(), lr = ln & 15, lg = ln >> 4;
  char* ob = ows + (size_t)bid * OW_B + gq * OH_B;
  lds_char_p xo = lds_at(ldsb + SLOT_B + gq * SLOT_B);
  const unsigned srcoff = (2 * w + (lg >> 1)) * 1024 + (16 * (lg & 1) + lr) * 16;
  const unsigned ooff = (2 * w + (lg & 1)) * 4096 + (lg >> 1) * 256 + lr * 16, ooff9 = 8 * 4096 + w * 1024 + ln * 16;
  const unsigned vwo = A.vk0;
  const int hp = A.hp;
  bf16x8 of[9], wa[4][8];
  lds_char_p wrd = lds_at(A.ldso + 64 * (16 * (lg >> 1) + 4 * (lg & 1) + ((ln >> 2) & 3)) + 32 * (lg & 1) + 8 * (ln & 3));
  lds_char_p wrd1 = lds_at((unsigned)(uintptr_t)wrd ^ 32);
#define WO_TAKE(j) do { FOR_I(8) wa[j][i] = tr_frag(((i & 1) ? wrd1 : wrd) + 2048 * (i >> 1), 512); END_FOR WAIT_LGKM(0); } while (0)
#define WO_NEXT(j) FOR_I(8) dma_wo<i>(A, (hp + (j)) & 3, vwo); END_FOR
  {
    f32x2 st[4];
    f32x4 u0[4], u1[4];
    st[0] = f32x2{A.c9, xg_sum(A.ls9)};
    wait_vm<8>();
    FOR_I(8) LDS_RD(f32x4, xo + (w * 8 + i) * 1024 + ln * 16) = A.o9[i]; END_FOR
    MEM_FENCE();
    u0[0] = LDS_RD(f32x4, xo + w * 8192 + srcoff); u1[0] = LDS_RD(f32x4, xo + w * 8192 + srcoff + 512);
    const float inv = 1.0f / xh_sum(A.ls);
    FOR_I(8) of[i] = pack8s<8 * (i & 1)>(A.o[i >> 1], inv); gstore16_dev(ob + ooff + 512 * i, of[i]); END_FOR
    WAIT_LGKM(0);
    LDS_RD(f32x2, xo + w * 10240 + ln * 8) = st[0];
    BARRIER();
    FOR_I(3)
      const int j = (w + 1 + i) & 3;
      st[1 + i] = LDS_RD(f32x2, xo + j * 10240 + ln * 8);
      u0[1 + i] = LDS_RD(f32x4, xo + j * 8192 + srcoff); u1[1 + i] = LDS_RD(f32x4, xo + j * 8192 + srcoff + 512);
    END_FOR
    const float nmin = fminf(fminf(st[0][0], st[1][0]), fminf(st[2][0], st[3][0]));
    float f[4], l = 0.f;
    FOR_I(4) f[i] = __builtin_amdgcn_exp2f(nmin - st[i][0]); l += f[i] * st[i][1]; END_FOR
    const float inv9 = 1.0f / l;
    f32x4 t0 = {0, 0, 0, 0}, t1 = {0, 0, 0, 0};
    FOR_I(4) t0 += u0[i] * (f[i] * inv9); t1 += u1[i] * (f[i] * inv9); END_FOR
    of[8] = pack8(t0, t1);
    gstore16_dev(ob + ooff9, of[8]);
  }
  wait_vm<9>();
  WO_TAKE(0);
  WO_NEXT(1)
  BARRIER();
  FOR_I(8) LDS_RD(bf16x8, lds_at(ldsb + gq * OH_B + ooff) + 512 * i) = of[i]; END_FOR
  LDS_RD(bf16x8, lds_at(ldsb + gq * OH_B + ooff9)) = of[8];
  STAMP(5);
  wait_vm<0>();
  BARRIER();
  if (wv == 0 && ln == 0) gstore8_dev(ows + FLAG_OFF + bid * 128, u32x2{tag_lo, tag_hi});
  STAMP(6);
  f32x4 acc[9][2];
  FOR_I(18) acc[i >> 1][i & 1] = f32x4{0, 0, 0, 0}; END_FOR
  auto oproj = [&](auto jc_, auto s0c_, auto nsc_, unsigned region) __attribute__((always_inline)) {
    constexpr int j = decltype(jc_)::value, S0 = decltype(s0c_)::value, NF = 9 * decltype(nsc_)::value, AH = 5;
    lds_char_p src = lds_at(ldsb + region + gq * OH_B + ln * 16);
    bf16x8 bq[NF];
    FOR_I(AH) bq[i] = LDS_RD(bf16x8, src + (i % 9) * 4096 + (S0 + i / 9) * 1024); END_FOR
    SCHED_FENCE();
    FOR_I(NF)
      acc[i % 9][0] = mfma32(wa[j][2 * (S0 + i / 9)], bq[i], acc[i % 9][0]);
      if constexpr (i + AH < NF) bq[i + AH] = LDS_RD(bf16x8, src + ((i + AH) % 9) * 4096 + (S0 + (i + AH) / 9) * 1024);
      acc[i % 9][1] = mfma32(wa[j][2 * (S0 + i / 9) + 1], bq[i], acc[i % 9][1]);
      SCHED_FENCE();
    END_FOR
  };
#define IC(n) std::integral_constant<int, n>{}
#define O_FETCH(j, region) FOR_I(9) dma_dev(ldsb + (region) + wv * 9216 + 1024 * i, ows + (size_t)(bid + 8 * (((hp + (j)) & 3) - hp)) * OW_B + wv * 9216 + 1024 * i, ln * 16); END_FOR
  WO_TAKE(1);
  WO_NEXT(2)
  oproj(IC(0), IC(0), IC(2), 0);
  wait_vm<0>();
  WO_TAKE(2);
  WO_NEXT(3)
  oproj(IC(0), IC(2), IC(2), 0);
  wait_vm<0>();
  WO_TAKE(3);
  const char* flags = ows + FLAG_OFF;
  if (bid == 0 && wv == 7) {
#pragma unroll 1
    for (int spin = 0; spin < SPIN_MAX; ++spin) {
      u32x2 f0, f1, f2, f3;
      asm volatile("global_load_dwordx2 %0, %4, %5 sc1\n\tglobal_load_dwordx2 %1, %4, %5 offset:128 sc1\n\t"
                   "global_load_dwordx2 %2, %4, %5 offset:256 sc1\n\tglobal_load_dwordx2 %3, %4, %5 offset:384 sc1\n\ts_waitcnt vmcnt(0)"
                   : "=&v"(f0), "=&v"(f1), "=&v"(f2), "=&v"(f3) : "v"(ln * 512), "s"(flags) : "memory");
      const unsigned dl = (f0[0] ^ tag_lo) | (f1[0] ^ tag_lo) | (f2[0] ^ tag_lo) | (f3[0] ^ tag_lo);
      const unsigned dh = (f0[1] ^ tag_hi) | (f1[1] ^ tag_hi) | (f2[1] ^ tag_hi) | (f3[1] ^ tag_hi);
      if (__all((dl | dh) == 0)) break;
      __builtin_amdgcn_s_sleep(2);
    }
    if (ln == 0) gstore8_dev(ows + GEN_OFF, u32x2{tag_lo, tag_hi});
  } else {
    poll_tag(flags, (bid + 8 * ((ln & 3) - hp)) * 128, tag_lo, tag_hi);
  }
  ACQUIRE();
  STAMP(7);
  BARRIER();
  O_FETCH(1, OW_B)
  O_FETCH(2, 0)
  wait_vm<9>();
  BARRIER();
  oproj(IC(1), IC(0), IC(4), OW_B);
  wait_vm<0>();
  BARRIER();
  O_FETCH(3, OW_B)
  oproj(IC(2), IC(0), IC(4), 0);
  wait_vm<0>();
  BARRIER();
  oproj(IC(3), IC(0), IC(4), OW_B);
  STAMP(8);
  f32x4 res[9];
  FOR_I(9) LDS_RD(f32x4, lds_at(ldsb + wv * 9216 + ln * 16) + 1024 * i) = gq ? acc[i][0] : acc[i][1]; res[i] = gq ? acc[i][1] : acc[i][0]; END_FOR
  BARRIER();
  FOR_I(9) res[i] += LDS_RD(f32x4, lds_at(ldsb + (wv ^ 4) * 9216 + ln * 16) + 1024 * i); END_FOR
  if (w != hg) {
    char* dst = ows + PART_OFF + (size_t)bid * OW_B + wv * 9216 + ln * 16;
    FOR_I(9) gstore16_dev(dst + 1024 * i, BF8(res[i])); END_FOR
    wait_vm<0>();
    if (ln == 0) gstore8_dev(ows + FLAG2_OFF + (bid * 8 + wv) * 128, u32x2{tag_lo, tag_hi});
  } else {
    const int o3 = (hg + 1 + (ln & 3) % 3) & 3;
    poll_tag(ows + FLAG2_OFF, ((bid - hg + o3) * 8 + wv) * 128, tag_lo, tag_hi);
    ACQUIRE();
    bf16x8 in[27];
    FOR_I(27) gload16_dev(in[i], ows + PART_OFF + (size_t)(bid - hg + ((hg + 1 + i / 9) & 3)) * OW_B + wv * 9216 + 1024 * (i % 9), ln * 16); END_FOR
    wait_vm<0>();
    FOR_I(27) pin(in[i]); res[i % 9] += __builtin_bit_cast(f32x4, in[i]); END_FOR
    char* outp = out + ((size_t)(b * SQ + lr) * (2 * DM) + br * DM + 128 * hp + 32 * w + 16 * gq + 4 * lg) * 2;
    FOR_I(9) *(u32x2*)(outp + (size_t)(16 * i) * (2 * DM * 2)) = u32x2{pk2(res[i][0], res[i][1]), pk2(res[i][2], res[i][3])}; END_FOR
  }
#undef WO_TAKE
#undef WO_NEXT
#undef O_FETCH
#undef IC
  STAMP(9);
  DIAG_FLUSH(10, ows, gq * 1024 + blockIdx.x * 4 + w);
}

extern "C" void kernel_launch(void* const* d_in, const int* in_sizes, int n_in,
                              void* d_out, int out_size, void* d_ws, size_t ws_size,
                              hipStream_t stream) {
  (void)in_sizes; (void)n_in; (void)out_size; (void)ws_size;
  (void)hipFuncSetAttribute((const void*)fused_kernel, hipFuncAttributeMaxDynamicSharedMemorySize, LDS1);
  hipStreamCaptureStatus cs = hipStreamCaptureStatusNone;
  (void)hipStreamIsCapturing(stream, &cs);
  fprintf(stderr, "PTRS cap=%d:", (int)cs);
  for (int i = 0; i < n_in; ++i) fprintf(stderr, " %p", d_in[i]);
  fprintf(stderr, " out %p ws %p\n", d_out, d_ws);
  const int reps = cs == hipStreamCaptureStatusNone ? 8 : 1;
  for (int rep = 0; rep < reps; ++rep) {
    if (reps > 1 && rep > 0 && rep < 6) { (void)hipMemsetAsync(d_ws, rep, ws_size, stream); (void)hipStreamSynchronize(stream); }
  hipLaunchKernelGGL(fused_kernel, dim3(256), dim3(512), LDS1, stream,
                     (const char*)d_in[0], (const char*)d_in[1], (const char*)d_in[2], (const char*)d_in[3], (const char*)d_in[4],
                     (const char*)d_in[5], (const char*)d_in[6], (const char*)d_in[7], (const char*)d_in[8], (const char*)d_in[9],
                     (char*)d_ws, (char*)d_out);

    if (reps > 1) {
      (void)hipStreamSynchronize(stream);
      std::vector<unsigned> h(2048 * 64);
      (void)hipMemcpy(h.data(), (char*)d_ws + DIAG_BASE, h.size() * 4, hipMemcpyDeviceToHost);
      unsigned t0 = ~0u; for (int i = 0; i < 2048; ++i) t0 = std::min(t0, h[i * 64]);
      fprintf(stderr, "rep %d stamps med(max):", rep);
      for (int k = 0; k < 10; ++k) { std::vector<unsigned> v; for (int i = 0; i < 2048; ++i) v.push_back(h[i * 64 + 2 * k] - t0); std::sort(v.begin(), v.end()); fprintf(stderr, " %.1f(%.1f)", v[1024] / 100.f, v[2047] / 100.f); }
      fprintf(stderr, " | steps:");
      for (int k = 0; k < 8; ++k) { std::vector<unsigned> v; for (int i = 0; i < 2048; ++i) v.push_back(h[i * 64 + 40 + k] - t0); std::sort(v.begin(), v.end()); fprintf(stderr, " %.1f", v[1024] / 100.f); }
      fprintf(stderr, "\n");
    }
  }
}
```

```cpp
#include <hip/hip_runtime.h>
#include <hip/hip_bf16.h>
#include <stdint.h>
#include <utility>

typedef short    s16x4  __attribute__((ext_vector_type(4)));
typedef __bf16   bf16x2 __attribute__((ext_vector_type(2)));
typedef __bf16   bf16x8 __attribute__((ext_vector_type(8)));
typedef float    f32x2  __attribute__((ext_vector_type(2)));
typedef float    f32x4  __attribute__((ext_vector_type(4)));
typedef unsigned u32x2  __attribute__((ext_vector_type(2)));
typedef unsigned u32x4  __attribute__((ext_vector_type(4)));
typedef __attribute__((address_space(3))) s16x4* lds_s16x4_p;
typedef __attribute__((address_space(3))) char* lds_char_p;

#define DI __device__ __forceinline__
#define PRIO(n) __builtin_amdgcn_s_setprio(n)

constexpr int SQ = 144, DM = 512, NH = 32, SK = 512, DH = 128, HD = NH * DH;
constexpr int TILE_B = 64 * 256;
constexpr int SLOT_B = 2 * TILE_B;
constexpr int LDS_B = 5 * SLOT_B;
constexpr int OV_B = 12 * 1024, PV_B = 3 * 1024, PART_OFF = 6 * OV_B, PAD_OFF = PART_OFF + 12 * PV_B, WG_B = PAD_OFF + 128;
constexpr size_t FLAG_OFF = (size_t)256 * WG_B, FLAG2_OFF = FLAG_OFF + 512 * 128, GEN_OFF = FLAG2_OFF + 256 * 3 * 128;
constexpr int SPIN_MAX = 1 << 18;
constexpr float QSCALE = 0.08838834764831845f * 1.4426950408889634f;
constexpr float RESCALE_THR = 8.0f;

template <class F, int... Is> DI void static_for_impl(F&& f, std::integer_sequence<int, Is...>) { (f(std::integral_constant<int, Is>{}), ...); }
template <int N, class F> DI void static_for(F&& f) { static_for_impl(f, std::make_integer_sequence<int, N>{}); }
#define FOR_I(N) static_for<N>([&](auto ic_) __attribute__((always_inline)) { constexpr int i = decltype(ic_)::value;
#define FOR_J(N) static_for<N>([&](auto jc_) __attribute__((always_inline)) { constexpr int j = decltype(jc_)::value;
#define END_FOR });
#define IC(n) std::integral_constant<int, n>{}

DI f32x4 mfma(bf16x8 a, bf16x8 b, f32x4 c) { f32x4 d = __builtin_amdgcn_mfma_f32_16x16x32_bf16(a, b, c, 0, 0, 0); asm volatile("" :: "v"(d)); return d; }
DI unsigned pk2(float lo, float hi) { f32x2 v = {lo, hi}; return __builtin_bit_cast(unsigned, __builtin_convertvector(v, bf16x2)); }
DI s16x4 tr_read(lds_char_p p) { return __builtin_amdgcn_ds_read_tr16_b64_v4i16((lds_s16x4_p)p); }
DI bf16x8 tr_frag(lds_char_p p, int second) {
  s16x4 lo = tr_read(p), hi = tr_read(p + second);
  return __builtin_bit_cast(bf16x8, __builtin_shufflevector(lo, hi, 0, 1, 2, 3, 4, 5, 6, 7));
}
DI bf16x8 pack8(f32x4 a, f32x4 b) {
  u32x4 w = {pk2(a[0], a[1]), pk2(a[2], a[3]), pk2(b[0], b[1]), pk2(b[2], b[3])};
  return __builtin_bit_cast(bf16x8, w);
}
DI float max3(float a, float b, float c) { return __builtin_fmaxf(__builtin_fmaxf(a, b), c); }
DI float max8(float m, f32x4 a, f32x4 b) { return max3(max3(max3(max3(m, a[0], a[1]), a[2], a[3]), b[0], b[1]), b[2], b[3]); }
DI float xg_max(float x) {
  auto a = __builtin_amdgcn_permlane32_swap(__float_as_uint(x), __float_as_uint(x), false, false);
  x = fmaxf(__uint_as_float(a[0]), __uint_as_float(a[1]));
  auto b = __builtin_amdgcn_permlane16_swap(__float_as_uint(x), __float_as_uint(x), false, false);
  return fmaxf(__uint_as_float(b[0]), __uint_as_float(b[1]));
}
DI float xg_sum(float x) {
  auto a = __builtin_amdgcn_permlane32_swap(__float_as_uint(x), __float_as_uint(x), false, false);
  x = __uint_as_float(a[0]) + __uint_as_float(a[1]);
  auto b = __builtin_amdgcn_permlane16_swap(__float_as_uint(x), __float_as_uint(x), false, false);
  return __uint_as_float(b[0]) + __uint_as_float(b[1]);
}

DI void dma1(unsigned lds, const char* base, unsigned v) {
  lds = __builtin_amdgcn_readfirstlane(lds);
  asm volatile("s_mov_b32 m0, %0\n\ts_nop 0\n\tglobal_load_lds_dwordx4 %2, %1" :: "s"(lds), "s"(base), "v"(v) : "memory");
}
DI void dma4(unsigned lds, const char* base, unsigned v0, unsigned v1, unsigned v2, unsigned v3) {
  lds = __builtin_amdgcn_readfirstlane(lds);
  asm volatile("s_mov_b32 m0, %0\n\ts_nop 0\n\t"
               "global_load_lds_dwordx4 %2, %1\n\tglobal_load_lds_dwordx4 %3, %1 offset:1024\n\t"
               "global_load_lds_dwordx4 %4, %1 offset:2048\n\tglobal_load_lds_dwordx4 %5, %1 offset:3072"
               :: "s"(lds), "s"(base), "v"(v0), "v"(v1), "v"(v2), "v"(v3) : "memory");
}
DI void dma4_nt(unsigned lds, const char* base, unsigned v0, unsigned v1, unsigned v2, unsigned v3) {
  lds = __builtin_amdgcn_readfirstlane(lds);
  asm volatile("s_mov_b32 m0, %0\n\ts_nop 0\n\t"
               "global_load_lds_dwordx4 %2, %1 nt\n\tglobal_load_lds_dwordx4 %3, %1 offset:1024 nt\n\t"
               "global_load_lds_dwordx4 %4, %1 offset:2048 nt\n\tglobal_load_lds_dwordx4 %5, %1 offset:3072 nt"
               :: "s"(lds), "s"(base), "v"(v0), "v"(v1), "v"(v2), "v"(v3) : "memory");
}
template <int OFF> DI void gload16_nt(bf16x8& dst, const char* base, unsigned voff) {
  asm volatile("global_load_dwordx4 %0, %1, %2 offset:%3 nt" : "=v"(dst) : "v"(voff), "s"(base), "i"(OFF) : "memory");
}
template <int OFF> DI void gload16(bf16x8& dst, const char* base, unsigned voff) {
  asm volatile("global_load_dwordx4 %0, %1, %2 offset:%3" : "=v"(dst) : "v"(voff), "s"(base), "i"(OFF) : "memory");
}
template <class T> DI void keep(T x) { asm volatile("" :: "v"(x)); }
DI void tie(float& x) { asm volatile("" : "+v"(x)); }
DI void pin(bf16x8& x) { asm volatile("" : "+v"(x)); }
DI void opaque(unsigned& x) { asm volatile("" : "+v"(x)); }
template <int OFF> DI void gload16_dev(bf16x8& dst, const char* base, unsigned voff) {
  asm volatile("global_load_dwordx4 %0, %1, %2 offset:%3 sc1" : "=v"(dst) : "v"(voff), "s"(base), "i"(OFF) : "memory");
}
DI void poll_tag(const char* base, unsigned voff, unsigned lo, unsigned hi) {
#pragma unroll 1
  for (int spin = 0; spin < SPIN_MAX; ++spin) {
    u32x2 f;
    asm volatile("global_load_dwordx2 %0, %1, %2 sc1\n\ts_waitcnt vmcnt(0)" : "=&v"(f) : "v"(voff), "s"(base) : "memory");
    if (__all(f[0] == lo && f[1] == hi)) break;
    __builtin_amdgcn_s_sleep(2);
  }
}
template <int OFF> DI void gstore16_dev(char* base, unsigned voff, bf16x8 v) {
  asm volatile("global_store_dwordx4 %0, %1, %2 offset:%3 sc1\n\ts_nop 1" :: "v"(voff), "v"(v), "s"(base), "i"(OFF) : "memory");
}
DI void gstore16_out(char* base, unsigned voff, u32x4 v) {
  asm volatile("global_store_dwordx4 %0, %1, %2 nt\n\ts_nop 1" :: "v"(voff), "v"(v), "s"(base) : "memory");
}
DI void gstore8_dev(char* p, u32x2 v) { asm volatile("global_store_dwordx2 %0, %1, off sc1" :: "v"(p), "v"(v) : "memory"); }
#define MEM_FENCE() asm volatile("" ::: "memory")
#define WAIT_LGKM(n) do { MEM_FENCE(); __builtin_amdgcn_s_waitcnt(0xC07F | ((n) << 8)); MEM_FENCE(); } while (0)
#define BARRIER() do { WAIT_LGKM(0); __builtin_amdgcn_s_barrier(); MEM_FENCE(); } while (0)
#define SCHED_FENCE() do { MEM_FENCE(); __builtin_amdgcn_sched_barrier(0); } while (0)
template <int N> DI void wait_vm() { asm volatile("s_waitcnt vmcnt(%0)" :: "i"(N < 63 ? N : 63) : "memory"); }
DI unsigned lane_id() {
  unsigned ln;
  asm volatile("v_mbcnt_lo_u32_b32 %0, -1, 0\n\tv_mbcnt_hi_u32_b32 %0, -1, %0" : "=v"(ln));
  return ln;
}
DI lds_char_p lds_at(unsigned a) { return (lds_char_p)(uintptr_t)a; }
#define LDS_RD(T, p) (*(__attribute__((address_space(3))) T*)(p))
#define BF8(x) __builtin_bit_cast(bf16x8, x)

struct Attn {
  bf16x8 qf[3][4];
  f32x4 o[3][8];
  f32x4 c[3];
  float ls[3];
  f32x4 sE[3][2], sO[3][2];
  u32x4 pE[3];
  bf16x8 kc[4];
  unsigned rdk, rdv;
};
#define SV(S, m) S[(m) >> 3][((m) >> 2) & 1][(m) & 3]
#define PACK(S, P, n) do { P[(n) >> 2][(n) & 3] = pk2(SV(S, 2 * (n)), SV(S, 2 * (n) + 1)); keep(P[(n) >> 2][(n) & 3]); } while (0)
#define GAP_X(S, P, m) do { SV(S, m) = __builtin_amdgcn_exp2f(SV(S, m)); keep(SV(S, m)); \
    if constexpr ((m) >= 2 && (m) % 2 == 0) PACK(S, P, (m) / 2 - 1); SCHED_FENCE(); } while (0)
#define GAP_Y(S, N, m, domax) do { A.ls[(m) >> 3] += SV(S, m); tie(A.ls[(m) >> 3]); \
    if constexpr (domax && (m) < 18 && (m) % 3 != 2) { constexpr int n_ = (m) / 3 * 2 + (m) % 3; \
      mx = max3(mx, N[n_ % 3][n_ / 6][2 * (n_ / 3 & 1)], N[n_ % 3][n_ / 6][2 * (n_ / 3 & 1) + 1]); tie(mx); } \
    if constexpr (domax && (m) == 19) grow = __any(mx > RESCALE_THR); SCHED_FENCE(); } while (0)
#define K_RD(kt, s) LDS_RD(bf16x8, kn + 4096 * (kt) + 256 * (s))
#define V_RD(ks, dt) tr_frag((((dt) & 1) ? vo : ve) + 8192 * (ks) + 256 * ((dt) >> 1), 4096)
#define QK1(S, h, a, s) do { S[j][h] = mfma(a, A.qf[j][s], (s) ? S[j][h] : A.c[j]); SCHED_FENCE(); } while (0)
#define PV1(a, P, dt) do { A.o[j][dt] = mfma(a, BF8(P[j]), A.o[j][dt]); SCHED_FENCE(); } while (0)
DI void rescale(Attn& A, f32x4 (&s)[3][2]) {
  FOR_J(3)
    const float d = xg_max(max8(0.f, s[j][0], s[j][1])), a = __builtin_amdgcn_exp2f(-d);
    A.c[j] -= d; s[j][0] -= d; s[j][1] -= d; A.ls[j] *= a;
    FOR_I(8) A.o[j][i] *= a; END_FOR
  END_FOR
}
DI void attn_x2a(Attn& A, lds_char_p kn, bf16x8* k2) {
  FOR_I(4) FOR_J(3)
    QK1(A.sO, 0, k2[i], i);
    if constexpr (j == 2) A.kc[i] = K_RD(3, i);
    GAP_X(A.sE, A.pE, 3 * i + j);
  END_FOR END_FOR
}
template <bool LAST> DI void attn_step(Attn& A, int sk, bool mid) {
  const int sv = sk == 4 ? 0 : sk + 1, sn = sv == 4 ? 0 : sv + 1;
  lds_char_p ve = lds_at(A.rdv + sv * SLOT_B), vo = lds_at((A.rdv + sv * SLOT_B) ^ 32);
  lds_char_p kn = lds_at(A.rdk + sn * SLOT_B);
  BARRIER();
  bf16x8 va[8], vb[8], k0[8], k2[4];
  u32x4 pO[3];
  float mx = 0.f;
  bool grow;
  FOR_I(4) FOR_J(3)
    QK1(A.sO, 1, A.kc[i], i);
    if constexpr (j == 2) va[i] = V_RD(0, i);
    GAP_X(A.sE, A.pE, 12 + 3 * i + j);
  END_FOR END_FOR
  FOR_I(8) FOR_J(3)
    PV1(va[i], A.pE, i);
    if constexpr (i == 0 && j == 0) PACK(A.sE, A.pE, 11);
    if constexpr (j == 2) {
      if constexpr (i < 4) va[4 + i] = V_RD(0, 4 + i);
      else if constexpr (!LAST) k0[i - 4] = K_RD(0, i - 4);
    }
    GAP_Y(A.sE, A.sO, 3 * i + j, true);
  END_FOR END_FOR
  if (__builtin_expect(grow, 0)) rescale(A, A.sO);
  SCHED_FENCE();
  FOR_I(8) FOR_J(3)
    if constexpr (!LAST) QK1(A.sE, i >> 2, k0[i], i & 3);
    if constexpr (j == 2) {
      if constexpr (i >= 4) vb[i - 4] = V_RD(1, i - 4);
      else if constexpr (!LAST) k0[4 + i] = K_RD(1, i);
      if constexpr (!LAST && i == 3) { if (mid) __builtin_amdgcn_s_barrier(); }
    }
    GAP_X(A.sO, pO, 3 * i + j);
  END_FOR END_FOR
  mx = 0.f;
  FOR_I(8) FOR_J(3)
    PV1(vb[i], pO, i);
    if constexpr (i == 0 && j == 0) PACK(A.sO, pO, 11);
    if constexpr (j == 2) {
      if constexpr (i < 4) vb[4 + i] = V_RD(1, 4 + i);
      else if constexpr (!LAST) k2[i - 4] = K_RD(2, i - 4);
    }
    GAP_Y(A.sO, A.sE, 3 * i + j, !LAST);
  END_FOR END_FOR
  if constexpr (!LAST) {
    if (__builtin_expect(grow, 0)) rescale(A, A.sE);
    SCHED_FENCE();
    attn_x2a(A, kn, k2);
  }
}

constexpr int NB2 = 4, NI = 23 + NB2;
constexpr int QP_W[NI] = {2, 2, 2, 0, 2, 0, 2, 0, 2, 0, 1, 0, 0, 0, 0, 0};
constexpr int QP_X[NI] = {1, 0, 1, 0, 1, 0, 1, 0, 1, 0, 1, 0, 0, 0, 0, 0};
constexpr int QP_E[2][NI] = {{0, 0, 0, 2, 1, 2, 1, 2, 1, 2, 1, 0, 3, 3, 2, 6, 6, 7, 7, 7, 7, 4, 0},
                             {0, 0, 0, 1, 1, 1, 1, 1, 1, 1, 2, 3, 1, 2, 2, 4, 3, 3, 3, 3, 4, 6, 9, 4, 2, 5, 1}};
constexpr int step_at(int g, int t) { return g == 0 ? 16 + t : t < NB2 ? 16 + 2 * t : 16 + NB2 + t; }
constexpr int tile_free(int g, int j) { return j == 0 ? 11 : j == 1 || j == 3 ? 13 : j < 5 ? 15 : step_at(g, (j - 4) / 2); }
constexpr int tile_last(int g, int j) { return j == 0 ? 14 : step_at(g, (j - 1) / 2) - 1; }
struct Sched {
  int w0[NI + 1], x0[NI + 1], e0[NI + 1], cum[NI + 1];
  int need[NI];
  int end[64], buf[64], dump[64];
  bool ok;
};
constexpr Sched make_sched(int g) {
  Sched S{};
  int endw[16] = {17, 25, 42}, endx[8] = {9, 34}, busy[12] = {}, w = 3, x = 2, e = 0, n = 42, nreg = 0;
  S.ok = true;
  for (int s = 0; s < NI; ++s) {
    S.w0[s] = w; S.x0[s] = x; S.e0[s] = e; S.cum[s] = n;
    for (int i = 0; i < QP_X[s]; ++i, ++x) { if (x >= 3 && 2 * (x - 3) + 1 > s) S.ok = false; endx[x] = n += 9; }
    for (int i = 0; i < QP_W[s]; ++i, ++w) { if (w > s + 6) S.ok = false; endw[w] = n += 8; }
    for (int i = 0; i < QP_E[g][s]; ++i, ++e) {
      const int fr = tile_free(g, e >> 2), la = tile_last(g, e >> 2), lag = s + (s < 15 ? 3 : 1);
      S.end[e] = n += 4;
      for (nreg = 0; nreg < 11 && busy[nreg] > s; ++nreg) {}
      S.buf[e] = s < fr ? nreg : -1;
      S.dump[e] = S.buf[e] < 0 ? -1 : (lag < la ? lag : la) > fr ? (lag < la ? lag : la) : fr;
      if (s > la) S.ok = false;
      if (S.buf[e] >= 0) { if (busy[S.buf[e]] > s) S.ok = false; busy[S.buf[e]] = S.dump[e]; }
    }
    int need = 0;
    if (s + 2 < 16) {
      if (w <= s + 2 || x <= (s + 2) >> 1) S.ok = false;
      need = endw[s + 2] > endx[(s + 2) >> 1] ? endw[s + 2] : endx[(s + 2) >> 1];
    }
    for (int k = 0; k < e; ++k) if (S.buf[k] < 0 && tile_last(g, k >> 2) <= s && S.end[k] > need) need = S.end[k];
    S.need[s] = need;
  }
  S.w0[NI] = w; S.x0[NI] = x; S.e0[NI] = e; S.cum[NI] = n;
  if (w != 16 || x != 8 || e != 64) S.ok = false;
  return S;
}
constexpr Sched SCH[2] = {make_sched(0), make_sched(1)};
static_assert(SCH[0].ok && SCH[1].ok, "loader schedule");
constexpr int XOFF = 3 * SLOT_B, XCH = SQ * 128;
constexpr int wpiece(int s) { return (((s + 2) % 6) >> 1) * SLOT_B + (s & 1) * 8192; }
constexpr int xpos(int c) { return ((c + 1) % 3) * XCH; }

__global__ __launch_bounds__(512, 2)
void fused_kernel(const char* __restrict__ xA, const char* __restrict__ kA, const char* __restrict__ vA, const char* __restrict__ wqA,
                  const char* __restrict__ woA, const char* __restrict__ xB, const char* __restrict__ kB, const char* __restrict__ vB,
                  const char* __restrict__ wqB, const char* __restrict__ woB, char* ows, char* __restrict__ out) {
  extern __shared__ __attribute__((aligned(1024))) char smem_raw[];
  const int wv = __builtin_amdgcn_readfirstlane(threadIdx.x >> 6), w = wv & 3, gq = wv >> 2;
  const bool loader = wv == 3 || wv == 6;
  const int cw = wv == 7 ? 2 : w;
  const unsigned ldsb = (unsigned)(uintptr_t)(lds_char_p)smem_raw, lds0 = ldsb + gq * TILE_B;
  const int bid = blockIdx.x, xcd = bid & 7, idx = bid >> 3;
  const int br = xcd >> 2, hg = xcd & 3, hp = idx & 3, h = 8 * hg + 2 * hp + gq, b = idx >> 2;
  const char* wob = (br ? woB : woA) + (size_t)(1024 * hg + 128 * gq) * DM * 2 + hp * 256;
  const char* code0;
  int code_len;
  asm volatile("s_getpc_b64 %0\n.Lfused_code_begin:\n\ts_mov_b32 %1, .Lfused_code_end-.Lfused_code_begin" : "=s"(code0), "=s"(code_len));
  unsigned tag_lo, tag_hi;
  const int rot = (b + 2 * hp) & 7;
  {
    const unsigned l = lane_id();
    const char* x0 = (br ? xB : xA) + (size_t)b * SQ * DM * 2 + 128 * rot;
    const unsigned vx = (l >> 3) * (DM * 2) + 16 * ((l & 7) ^ (l >> 4));
    FOR_I(2) dma1(ldsb + XOFF + xpos(0) + (2 * wv + i) * 1024, x0 + (16 * wv + 8 * i) * DM * 2, vx ^ (64 * i)); END_FOR
    if (wv < 2) dma1(ldsb + XOFF + xpos(0) + (16 + wv) * 1024, x0 + (128 + 8 * wv) * DM * 2, vx ^ (64 * wv));
    const char* w0 = (br ? wqB : wqA) + h * DH * 2 + (size_t)(64 * rot + 8 * w) * HD * 2;
    const unsigned vw = ((l >> 2) & 3) * (HD * 2) + 64 * (l >> 4) + 16 * ((l & 3) ^ (2 * (w & 1)));
    FOR_I(2) dma1(lds0 + wpiece(0) + (2 * w + i) * 1024, w0 + (size_t)4 * i * HD * 2, vw); END_FOR
  }
  if (loader) {
    const unsigned ln = lane_id();
    u32x2 gen;
    asm volatile("global_load_dwordx2 %0, %1, %2 sc1" : "=v"(gen) : "v"(0), "s"(ows + GEN_OFF) : "memory");
    const int rk = (h + b) & 7;
    const char* xg = (br ? xB : xA) + (size_t)(b * SQ + 72 * gq) * DM * 2;
    const char* wg = (br ? wqB : wqA) + h * DH * 2;
    const char* kg = (br ? kB : kA) + (size_t)(b * NH + h) * SK * DH * 2;
    const char* vg = (br ? vB : vA) + (size_t)(b * NH + h) * SK * DH * 2;
    const unsigned vl = 64 * (ln >> 4) + 16 * (ln & 3), q4 = (ln >> 2) & 3;
    const unsigned vk = q4 * 256 + vl, vw = q4 * (HD * 2) + vl, vo = q4 * (DM * 2) + vl;
    const unsigned vx = (ln >> 3) * (DM * 2) + 16 * ((ln & 7) ^ (ln >> 4) ^ (4 * gq));
    unsigned vw4[4], vx4[4], vo4[4];
    FOR_I(4)
      vw4[i] = (vw ^ (16 * (i & 2))) + i * (4 * HD * 2 - 1024);
      vx4[i] = (vx ^ (64 * (i & 1))) + i * (8 * DM * 2 - 1024);
      vo4[i] = (vo ^ (32 * (i & 1))) + i * (4 * DM * 2 - 1024);
    END_FOR
    auto ld_w = [&](int s, int off) __attribute__((always_inline)) {
      const char* src = wg + (size_t)((s + 2 * rot) & 15) * 32 * HD * 2;
      FOR_I(2) dma4(lds0 + off + 4096 * i, src + (size_t)16 * i * HD * 2, vw4[0], vw4[1], vw4[2], vw4[3]); END_FOR
    };
    auto ld_x = [&](int c, int off) __attribute__((always_inline)) {
      const char* src = xg + 128 * ((c + rot) & 7);
      FOR_I(2) dma4(ldsb + XOFF + off + gq * 9216 + 4096 * i, src + 32 * i * DM * 2, vx4[0], vx4[1], vx4[2], vx4[3]); END_FOR
      dma1(ldsb + XOFF + off + gq * 9216 + 8192, src + 64 * DM * 2, vx4[0]);
    };
    auto ld_k = [&](int slot, int t, int q) __attribute__((always_inline)) {
      dma4_nt(lds0 + slot * SLOT_B + 4096 * q, kg + ((t + rk) & 7) * TILE_B + 4096 * q, vk, vk, vk ^ 32, vk ^ 32);
    };
    auto ld_v = [&](int slot, int t, int q) __attribute__((always_inline)) {
      dma4_nt(lds0 + slot * SLOT_B + 4096 * q, vg + ((t + rk) & 7) * TILE_B + 4096 * q, vk, vk ^ 32, vk, vk ^ 32);
    };
    auto ld_wo = [&](int u0, int u1) __attribute__((always_inline)) {
#pragma unroll 1
      for (int u = u0; u < u1; ++u) {
        const int r3 = (u - 4) / 3, row = u < 4 ? 256 * hp + 32 * u : 256 * ((hp + u - 3 - 3 * r3) & 3) + 32 * r3, ps = u < 8 ? u + 2 : u - 8;
        FOR_I(2) dma4(lds0 + (ps >> 1) * SLOT_B + (ps & 1) * 8192 + 4096 * i, wob + (size_t)(row + 16 * i) * DM * 2, vo4[0], vo4[1], vo4[2], vo4[3]); END_FOR
      }
    };
    bf16x8 er[48];
    unsigned tw0, tw1;
    PRIO(3);
    ld_w(1, wpiece(1));
    wait_vm<8>();
    asm volatile("" : "+v"(gen));
    tag_lo = __builtin_amdgcn_readfirstlane(gen[0]) + 1u;
    tag_hi = __builtin_amdgcn_readfirstlane(gen[1]);
    BARRIER();
    ld_x(1, xpos(1)); ld_w(2, wpiece(2));
    wait_vm<17>();
    auto stream = [&](auto gc_) __attribute__((always_inline)) {
    constexpr int G = decltype(gc_)::value, NG = step_at(G, 7);
#define SC SCH[G]
    static_for<NG>([&](auto sc_) __attribute__((always_inline)) {
      constexpr int s = decltype(sc_)::value;
      BARRIER();
      FOR_I(64)
        if constexpr (SC.dump[i] == s) {
          wait_vm<SC.cum[s] - SC.end[i]>();
          lds_char_p dst = lds_at(lds0 + (i >> 2) % 5 * SLOT_B + (i & 3) * 4096 + ln * 16);
          FOR_J(4) pin(er[4 * SC.buf[i] + j]); LDS_RD(bf16x8, dst + 1024 * j) = er[4 * SC.buf[i] + j]; END_FOR
        }
      END_FOR
      FOR_I(QP_X[s]) ld_x(SC.x0[s] + i, xpos(SC.x0[s] + i)); END_FOR
      FOR_I(QP_W[s]) ld_w(SC.w0[s] + i, wpiece(SC.w0[s] + i)); END_FOR
      FOR_I(QP_E[G][s])
        constexpr int e = SC.e0[s] + i, q = e & 3, isv = (e >> 2) & 1;
        if constexpr (SC.buf[e] >= 0) {
          WAIT_LGKM(0);
          const char* src = (isv ? vg : kg) + (((e >> 3) + rk) & 7) * TILE_B + 4096 * q;
          FOR_J(4) gload16_nt<1024 * j>(er[4 * SC.buf[e] + j], src, vk ^ (isv ? 32 * (j & 1) : 16 * (j & 2))); END_FOR
        } else if constexpr (isv) ld_v((e >> 2) % 5, e >> 3, q);
        else ld_k((e >> 2) % 5, e >> 3, q);
      END_FOR
      if constexpr (s < NG - 1) wait_vm<SC.cum[s + 1] - SC.need[s]>();
      if constexpr (s == NG - 2) {
        unsigned d0, d1;
        const char* src = (br ? woB : woA) + (size_t)(hg * 1024 + (idx >> 3) * 256 + gq * 128 + (idx & 7) * 16) * DM * 2;
        asm volatile("global_load_dword %0, %2, %3\n\tglobal_load_dword %1, %2, %4" : "=&v"(d0), "=&v"(d1) : "v"(ln * 128), "s"(src), "s"(src + 8192) : "memory");
        tw0 = d0; tw1 = d1;
      }
    });
    ld_wo(0, 4);
    wait_vm<32>();
    keep(tw0); keep(tw1);
#undef SC
    };
    if (gq) stream(IC(1)); else stream(IC(0));
    BARRIER();
    ld_wo(4, 8);
    if (!gq) BARRIER();
    wait_vm<0>();
    BARRIER();
    if (ln == 0) gstore8_dev(ows + FLAG_OFF + (bid * 2 + gq) * 128, u32x2{tag_lo, tag_hi});
    ld_wo(8, 10);
    poll_tag(ows + FLAG_OFF, ((bid + 8 * ((int)(ln & 3) - hp)) * 2 + gq) * 128, tag_lo, tag_hi);
    BARRIER();
    {
      if (bid == 0 && gq) {
#pragma unroll 1
        for (int spin = 0; spin < SPIN_MAX; ++spin) {
          u32x2 f0, f1, f2, f3;
          asm volatile("global_load_dwordx2 %0, %4, %5 sc1\n\tglobal_load_dwordx2 %1, %4, %5 offset:256 sc1\n\t"
                       "global_load_dwordx2 %2, %4, %5 offset:512 sc1\n\tglobal_load_dwordx2 %3, %4, %5 offset:768 sc1\n\ts_waitcnt vmcnt(0)"
                       : "=&v"(f0), "=&v"(f1), "=&v"(f2), "=&v"(f3) : "v"(ln * 1024), "s"(ows + FLAG_OFF) : "memory");
          const unsigned dl = (f0[0] ^ tag_lo) | (f1[0] ^ tag_lo) | (f2[0] ^ tag_lo) | (f3[0] ^ tag_lo);
          const unsigned dh = (f0[1] ^ tag_hi) | (f1[1] ^ tag_hi) | (f2[1] ^ tag_hi) | (f3[1] ^ tag_hi);
          if (__all((dl | dh) == 0)) break;
          __builtin_amdgcn_s_sleep(2);
        }
        if (ln == 0) gstore8_dev(ows + GEN_OFF, u32x2{tag_lo, tag_hi});
      }
    }
    BARRIER();
    ld_wo(10, 14);
    bf16x8 last[16];
    FOR_I(4) FOR_J(4) gload16<1024 * j>(last[4 * i + j], wob + (size_t)(256 * ((hp + 2 + (i >> 1)) & 3) + 96 + 16 * (i & 1)) * DM * 2, vo4[j]); END_FOR END_FOR
    wait_vm<0>();
    BARRIER();
    FOR_I(16) pin(last[i]); LDS_RD(bf16x8, lds_at(lds0 + 3 * SLOT_B + ln * 16) + 1024 * i) = last[i]; END_FOR
    BARRIER();
  } else {
    const unsigned ln = lane_id(), r = ln & 15, g = ln >> 4, q4 = (ln >> 2) & 3, p4 = ln & 3;
    Attn A;
    {
      u32x2 gen;
      unsigned dummy = 0;
      if (wv == 0) {
        const int off = max(min((idx * 16 + (int)(ln & 15)) * 128, code_len - 4), 0);
        asm volatile("global_load_dword %0, %1, %2" : "+v"(dummy) : "v"(off), "s"(code0) : "memory");
      }
      if (wv == 1) {
        const unsigned e = ln & 7, top = ln & 4;
        const char* kv = (size_t)(b * NH + h) * SK * DH * 2 + (top ? 2 * SK * DH * 2 - 128 : 0) + ((ln & 1) ? (br ? vB : vA) : (br ? kB : kA));
        const char* wr = (ln & 1) ? wob + (top ? 1023 * DM * 2 : 0)
                                  : out + ((size_t)(b * SQ + (top ? SQ - 1 : 0)) * (2 * DM) + br * DM + 128 * hp + 32 * hg) * 2;
        const char* pw = ows + (size_t)(top ? bid - hg + (int)(ln & 3) : bid + 8 * ((int)(ln & 3) - hp)) * WG_B + PAD_OFF;
        const char* pq = (br ? wqB : wqA) + h * DH * 2 + (size_t)(73 * e) * HD * 2;
        const char* p = (ln & 16) ? pq : (ln & 8) ? pw : (ln & 2) ? wr : kv;
        asm volatile("global_load_dword %0, %1, off" : "+v"(dummy) : "v"(p) : "memory");
      }
      asm volatile("global_load_dwordx2 %0, %1, %2 sc1\n\ts_waitcnt vmcnt(0)" : "=&v"(gen) : "v"(0), "s"(ows + GEN_OFF) : "memory");
      keep(dummy);
      tag_lo = __builtin_amdgcn_readfirstlane(gen[0]) + 1u;
      tag_hi = __builtin_amdgcn_readfirstlane(gen[1]);
      asm volatile("" : "+s"(tag_lo), "+s"(tag_hi));
    }
    {
      unsigned xr[2], rw[2];
      xr[0] = ldsb + XOFF + (48 * cw + r) * 128 + 16 * (g ^ (r >> 1)); xr[1] = xr[0] ^ 64;
      rw[0] = lds0 + 2048 * g + 64 * q4 + 16 * (p4 ^ (2 * (g & 1))); rw[1] = rw[0] + 2 * SLOT_B;
      FOR_I(2) opaque(rw[i]); END_FOR
      FOR_I(2) opaque(xr[i]); END_FOR
#define X_RD(set, s, i) xf[set][i] = LDS_RD(bf16x8, lds_at(xr[(s) & 1]) + (xpos((s) >> 1) + 2048 * (i)))
#define W_RD(set, s, i) wf[set][i] = tr_frag(lds_at(rw[wpiece(s) >= 2 * SLOT_B]) + (256 * ((i) >> 1) + 8 * ((i) & 1) + wpiece(s) % (2 * SLOT_B)), 1024)
      bf16x8 xf[2][3], wf[2][8];
      FOR_I(24) A.o[i >> 3][i & 7] = f32x4{0, 0, 0, 0}; END_FOR
      BARRIER();
      FOR_I(3) X_RD(0, 0, i); END_FOR
      FOR_I(8) W_RD(0, 0, i); END_FOR
      static_for<16>([&](auto sc_) __attribute__((always_inline)) {
        constexpr int s = decltype(sc_)::value, cs = s & 1, ns = cs ^ 1;
        BARRIER();
        SCHED_FENCE();
        FOR_I(8)
          if constexpr (i % 2 == 0) PRIO(3 - i / 2);
          FOR_J(3) A.o[j][i] = mfma(wf[cs][i], xf[cs][j], A.o[j][i]); END_FOR
          if constexpr (s < 15) {
            if constexpr (i < 6) W_RD(ns, s + 1, i);
            if constexpr (i < 3) X_RD(ns, s + 1, i);
            else if constexpr (i < 5) W_RD(ns, s + 1, 3 + i);
          }
          SCHED_FENCE();
        END_FOR
      });
      FOR_I(12) A.qf[i >> 2][i & 3] = pack8(A.o[i >> 2][2 * (i & 3)] * QSCALE, A.o[i >> 2][2 * (i & 3) + 1] * QSCALE); END_FOR
#undef X_RD
#undef W_RD
    }
    if (gq) PRIO(1); else PRIO(2);
    A.rdk = lds0 + 1024 * (r >> 2) + 64 * (r & 3) + 16 * (g ^ (2 * (r >> 3)));
    A.rdv = lds0 + 1024 * g + 64 * q4 + 32 * (g & 1) + 8 * p4;
    {
      lds_char_p kn = lds_at(A.rdk);
      bf16x8 k0[8], k2[4];
      FOR_I(8) k0[i] = K_RD(i >> 2, i & 3); END_FOR
      FOR_I(4) k2[i] = K_RD(2, i); END_FOR
      FOR_I(3) A.c[i] = f32x4{0, 0, 0, 0}; A.ls[i] = 0.f; END_FOR
      FOR_I(8) FOR_J(3) QK1(A.sE, i >> 2, k0[i], i & 3); END_FOR END_FOR
      FOR_I(24) A.o[i >> 3][i & 7] = f32x4{0, 0, 0, 0}; END_FOR
      FOR_J(3)
        const float m = xg_max(max8(A.sE[j][0][0], A.sE[j][0], A.sE[j][1]));
        A.c[j] -= m; A.sE[j][0] -= m; A.sE[j][1] -= m;
      END_FOR
      SCHED_FENCE();
      attn_x2a(A, kn, k2);
    }
    int sk = 0;
#pragma unroll 1
    for (int t = 0; t < 7; ++t) {
      asm volatile("" : "+s"(sk));
      attn_step<false>(A, sk, gq && t < NB2);
      sk = sk >= 3 ? sk - 3 : sk + 2;
    }
    attn_step<true>(A, sk, false);
    const unsigned l2 = lane_id(), v16 = l2 * 16;
    const int cv = 3 * gq + cw;
    char* ob = ows + (size_t)bid * WG_B + cv * OV_B;
    bf16x8 bqs[36];
    bf16x8* const bq[4] = {bqs, bqs + 9, bqs + 18, bqs + 27};
    {
      float inv[3];
      FOR_J(3) inv[j] = __builtin_amdgcn_rcpf(xg_sum(A.ls[j])); END_FOR
      FOR_I(4) FOR_J(3)
        if constexpr (i == 0 && j == 0) { SCHED_FENCE(); if (!gq) __builtin_amdgcn_s_barrier(); SCHED_FENCE(); }
        bq[2 + (i >> 1)][3 * (i & 1) + j] = pack8(A.o[j][2 * i] * inv[j], A.o[j][2 * i + 1] * inv[j]);
        gstore16_dev<1024 * j>(ob + 3072 * i, v16, bq[2 + (i >> 1)][3 * (i & 1) + j]);
      END_FOR END_FOR
    }
    if (!gq) PRIO(0);
    auto oproj = [&](auto u0_, auto nu_, bf16x8* bs, auto&& side) __attribute__((always_inline)) {
      constexpr int U0 = decltype(u0_)::value, NF = 8 * decltype(nu_)::value, AH = 4;
      unsigned ub[NF / 8];
#define WO_AT(f) (lds_at(ub[(f) >> 3] ^ (32 * ((f) & 1))) + 256 * (((f) & 7) >> 1))
#define WO_BASE(k) do { constexpr int ps_ = (U0 + (k) + 2) % 10; ub[k] = A.rdv + (ps_ >> 1) * SLOT_B + (ps_ & 1) * 8192; opaque(ub[k]); } while (0)
      s16x4 wl[NF], wh[NF];
      WO_BASE(0);
      FOR_I(AH) wl[i] = tr_read(WO_AT(i)); wh[i] = tr_read(WO_AT(i) + 4096); END_FOR
      SCHED_FENCE();
      FOR_I(NF)
        side(ic_);
        const bf16x8 a = BF8(__builtin_shufflevector(wl[i], wh[i], 0, 1, 2, 3, 4, 5, 6, 7));
        FOR_J(3)
          A.o[j][i & 7] = mfma(a, bs[3 * (i >> 3) + j], U0 == 0 && i < 8 ? f32x4{0, 0, 0, 0} : A.o[j][i & 7]);
          SCHED_FENCE();
          if constexpr (i + AH < NF) {
            if constexpr (j == 0) { if constexpr ((i + AH) % 8 == 0) WO_BASE((i + AH) / 8); wl[i + AH] = tr_read(WO_AT(i + AH)); }
            if constexpr (j == 1) wh[i + AH] = tr_read(WO_AT(i + AH) + 4096);
          }
          SCHED_FENCE();
        END_FOR
      END_FOR
#undef WO_AT
#undef WO_BASE
    };
#define SIDE(...) [&](auto kc_) __attribute__((always_inline)) { constexpr int k = decltype(kc_)::value; (void)k; __VA_ARGS__ }
    auto fetch = [&](auto rc_) __attribute__((always_inline)) {
      constexpr int r = decltype(rc_)::value;
      FOR_I(3)
        const char* src = ows + (size_t)(bid + 8 * (((hp + 1 + i) & 3) - hp)) * WG_B + cv * OV_B + 3072 * r;
        FOR_J(3) gload16_dev<1024 * j>(bq[r][3 * i + j], src, v16); END_FOR
      END_FOR
    };
    wait_vm<0>();
    BARRIER();
    oproj(IC(0), IC(2), bq[2], SIDE());
    BARRIER();
    fetch(IC(0)); fetch(IC(1));
    oproj(IC(2), IC(2), bq[3], SIDE());
    fetch(IC(2));
    BARRIER();
    wait_vm<18>();
    FOR_I(9) pin(bq[0][i]); END_FOR
    oproj(IC(4), IC(6), bq[0], SIDE(if constexpr (k == 24) { fetch(IC(3)); wait_vm<18>(); FOR_I(9) pin(bq[1][i]); END_FOR }));
    wait_vm<9>();
    FOR_I(9) pin(bq[2][i]); END_FOR
    BARRIER();
    oproj(IC(10), IC(3), bq[2], SIDE());
    wait_vm<0>();
    FOR_I(9) pin(bq[3][i]); END_FOR
    BARRIER();
    unsigned pb[3];
    FOR_I(3) pb[i] = ldsb + ((3 * cw + i) >> 1) * SLOT_B + ((3 * cw + i) & 1) * 8192 + v16; END_FOR
#define PART_AT(n) (lds_at(pb[(n) >> 3]) + ((n) & 7) * 1024)
    f32x4 other[24];
    oproj(IC(13), IC(3), bq[3], SIDE(other[k] = LDS_RD(f32x4, PART_AT(k));));
    if (!gq) {
      BARRIER();
      FOR_I(24) LDS_RD(f32x4, PART_AT(i)) = A.o[i >> 3][i & 7]; END_FOR
      BARRIER();
    } else {
      FOR_I(24) A.o[i >> 3][i & 7] += other[i]; END_FOR
      FOR_I(4)
        if (i != hg) {
          char* dst = ows + (size_t)bid * WG_B + PART_OFF + (i * 3 + cw) * PV_B;
          FOR_J(3) gstore16_dev<1024 * j>(dst, v16, pack8(A.o[j][2 * i], A.o[j][2 * i + 1])); END_FOR
        }
      END_FOR
      wait_vm<0>();
      if (l2 == 0) gstore8_dev(ows + FLAG2_OFF + (bid * 3 + cw) * 128, u32x2{tag_lo, tag_hi});
      f32x4 res[6];
      FOR_I(6)
        const f32x4 lo = (hg & 1) ? A.o[i >> 1][2 + (i & 1)] : A.o[i >> 1][i & 1], hi = (hg & 1) ? A.o[i >> 1][6 + (i & 1)] : A.o[i >> 1][4 + (i & 1)];
        res[i] = (hg & 2) ? hi : lo;
      END_FOR
      poll_tag(ows + FLAG2_OFF, ((bid - hg + (int)(l2 & 3)) * 3 + cw) * 128, tag_lo, tag_hi);
      bf16x8 in[9];
      FOR_I(3)
        const char* src = ows + (size_t)(bid - hg + ((hg + 1 + i) & 3)) * WG_B + PART_OFF + (hg * 3 + cw) * PV_B;
        FOR_J(3) gload16_dev<1024 * j>(in[3 * i + j], src, v16); END_FOR
      END_FOR
      wait_vm<0>();
      FOR_I(9)
        pin(in[i]);
        const u32x4 v = __builtin_bit_cast(u32x4, in[i]);
        res[2 * (i % 3)] += f32x4{__uint_as_float(v[0] << 16), __uint_as_float(v[0] & 0xffff0000u), __uint_as_float(v[1] << 16), __uint_as_float(v[1] & 0xffff0000u)};
        res[2 * (i % 3) + 1] += f32x4{__uint_as_float(v[2] << 16), __uint_as_float(v[2] & 0xffff0000u), __uint_as_float(v[3] << 16), __uint_as_float(v[3] & 0xffff0000u)};
      END_FOR
      char* outp = out + ((size_t)(b * SQ + 48 * cw) * (2 * DM) + br * DM + 128 * hp + 32 * hg) * 2;
      const unsigned vout = (l2 & 15) * (2 * DM * 2) + 16 * (l2 >> 4);
      FOR_J(3)
        auto p0 = __builtin_amdgcn_permlane32_swap(pk2(res[2 * j][0], res[2 * j][1]), pk2(res[2 * j + 1][0], res[2 * j + 1][1]), false, false);
        auto p1 = __builtin_amdgcn_permlane32_swap(pk2(res[2 * j][2], res[2 * j][3]), pk2(res[2 * j + 1][2], res[2 * j + 1][3]), false, false);
        auto r0 = __builtin_amdgcn_permlane16_swap(p0[0], p0[1], false, false);
        auto r1 = __builtin_amdgcn_permlane16_swap(p1[0], p1[1], false, false);
        const u32x4 v = {r0[0], r1[0], r0[1], r1[1]};
        gstore16_out(outp + (size_t)16 * j * (2 * DM * 2), vout, v);
      END_FOR
    }
#undef PART_AT
#undef SIDE
  }
  asm volatile(".Lfused_code_end:");
}

extern "C" void kernel_launch(void* const* d_in, const int* in_sizes, int n_in,
                              void* d_out, int out_size, void* d_ws, size_t ws_size,
                              hipStream_t stream) {
  (void)in_sizes; (void)n_in; (void)out_size; (void)ws_size;
  (void)hipFuncSetAttribute((const void*)fused_kernel, hipFuncAttributeMaxDynamicSharedMemorySize, LDS_B);
  hipLaunchKernelGGL(fused_kernel, dim3(256), dim3(512), LDS_B, stream,
                     (const char*)d_in[0], (const char*)d_in[1], (const char*)d_in[2], (const char*)d_in[3], (const char*)d_in[4],
                     (const char*)d_in[5], (const char*)d_in[6], (const char*)d_in[7], (const char*)d_in[8], (const char*)d_in[9],
                     (char*)d_ws, (char*)d_out);
}
```
